```python
import torch

def _softmax(x, axis=-1):
    return torch.softmax(x, dim=axis)
def _gelu(x):
    return torch.nn.functional.gelu(x, approximate="tanh")
def _silu(x):
    return torch.nn.functional.silu(x)
def _sigmoid(x):
    return torch.sigmoid(x)
def _layernorm(x, axis=-1, eps=1e-5):
    m = x.mean(dim=axis, keepdim=True)
    v = x.var(dim=axis, keepdim=True, unbiased=False)
    return (x - m) / torch.sqrt(v + eps)
def _scatter_add(tgt, idx, src, axis=0):
    return tgt.clone().index_add_(axis, idx, src)
def _segment_sum(src, seg, n):
    out = torch.zeros((n,) + src.shape[1:], dtype=src.dtype)
    return out.index_add_(0, seg, src)
def _pad(x, pw):
    flat = tuple(v for pair in reversed(pw) for v in pair)
    return torch.nn.functional.pad(x, flat)

kernel_name = 'interleave_layers__ssm_conv_h3__glu_gated_swiglu__prompt'

def setup_inputs():

    _w = (lambda a: (lambda t: t.to(torch.bfloat16) if t.is_floating_point() else t)(torch.as_tensor(a)))
    def _A_setup():
        B, S, D = 1, 2048, 576
        ks = 4
        return {
            "q": _w((lambda *s: torch.randn(*s, dtype=torch.bfloat16) * 0.05)(B, S, D)),
            "k": _w((lambda *s: torch.randn(*s, dtype=torch.bfloat16) * 0.2)(B, S, D)),
            "v": _w((lambda *s: torch.randn(*s, dtype=torch.bfloat16) * 0.2)(B, S, D)),
            "shift_k": _w((lambda *s: torch.randn(*s, dtype=torch.bfloat16) * 0.2)(D, ks) * 0.5),
            "ssm_k": _w((lambda *s: torch.randn(*s, dtype=torch.bfloat16) * 0.4)(D, S) * (1.0 / S ** 0.5)),
            "ssm_D": _w((lambda *s: torch.randn(*s, dtype=torch.bfloat16) * 0.2)(D)),
        }
    _A_in = _A_setup()

    def _B_setup():
        B, S, D, F = 1, 2048, 576, 4608
        return {
            "x": _w((lambda *s: torch.randn(*s, dtype=torch.bfloat16) * 0.05)(B, S, D)),
            "w1": _w((lambda *s: torch.randn(*s, dtype=torch.bfloat16) * 0.2)(D, F) * (2.0 / D) ** 0.5),
            "w3": _w((lambda *s: torch.randn(*s, dtype=torch.bfloat16) * 0.2)(D, F) * (2.0 / D) ** 0.5),
            "w2": _w((lambda *s: torch.randn(*s, dtype=torch.bfloat16) * 0.2)(F, D) * (2.0 / F) ** 0.5),
        }
    _B_in = _B_setup()

    return {
        "A_q": _A_in["q"],
        "A_k": _A_in["k"],
        "A_v": _A_in["v"],
        "A_shift_k": _A_in["shift_k"],
        "A_ssm_k": _A_in["ssm_k"],
        "A_ssm_D": _A_in["ssm_D"],
        "B_x": _B_in["x"],
        "B_w1": _B_in["w1"],
        "B_w3": _B_in["w3"],
        "B_w2": _B_in["w2"],
    }

def reference(A_q, A_k, A_v, A_shift_k, A_ssm_k, A_ssm_D, B_x, B_w1, B_w3, B_w2):
    def _A_ref(q, k, v, shift_k, ssm_k, ssm_D):
        B, S, D = q.shape
        ks = shift_k.shape[-1]
        k_pad = _pad(k, ((0, 0), (ks - 1, 0), (0, 0)))
        k_shift = torch.zeros_like(k)
        for t in range(ks):
            k_shift = k_shift + k_pad[:, t:t + S, :] * shift_k[:, ks - 1 - t][None, None, :]
        kv = k_shift * v
        L = 2 * S
        kv_pad = _pad(kv, ((0, 0), (0, S), (0, 0)))
        kf_pad = _pad(ssm_k, ((0, 0), (0, S)))
        kv_f = (lambda x, axes=(-2,-1): torch.fft.rfft2(x.float(), dim=axes))(kv_pad[:, None, :, :], axes=(1, 2))
        kf_f = (lambda x, axes=(-2,-1): torch.fft.rfft2(x.float(), dim=axes))(kf_pad[:, None, :], axes=(1, 2))
        kf_f = (lambda x, *d: x.permute(*d))(kf_f, 1, 2, 0)
        y_f = kv_f * kf_f[None, :, :, :]
        y = (lambda x, s, axes=(-2,-1): torch.fft.irfft2(x, s=s, dim=axes).to(torch.bfloat16))(y_f, (1, L), axes=(1, 2))[:, 0, :S, :]
        y = y + kv * ssm_D[None, None, :]
        return q * y

    def _B_ref(x, w1, w3, w2):
        gate = torch.einsum("bsd,df->bsf", x, w1)
        up = torch.einsum("bsd,df->bsf", x, w3)
        h = _silu(gate) * up
        out = torch.einsum("bsf,fd->bsd", h, w2)
        return out

    x = A_q
    x = x + _A_ref(_layernorm(x, axis=-1), A_k, A_v, A_shift_k, A_ssm_k, A_ssm_D)
    x = x + _B_ref(_layernorm(x, axis=-1), B_w1, B_w3, B_w2)
    x = x + _A_ref(_layernorm(x, axis=-1), A_k, A_v, A_shift_k, A_ssm_k, A_ssm_D)
    x = x + _B_ref(_layernorm(x, axis=-1), B_w1, B_w3, B_w2)
    return x
```

```cpp
#include <hip/hip_runtime.h>
#include <hip/hip_bf16.h>
#include <stdint.h>

typedef __attribute__((ext_vector_type(8))) short bf16x8;
typedef __attribute__((ext_vector_type(4))) short bf16x4;
typedef __attribute__((ext_vector_type(4))) float f32x4;
typedef __attribute__((ext_vector_type(4))) unsigned u32x4;
typedef __attribute__((ext_vector_type(2))) unsigned u32x2;
typedef unsigned short u16;

static constexpr int S_ = 2048, D_ = 576, F_ = 4608;
static constexpr int KT1 = D_ / 32;
static constexpr int KT2 = F_ / 32;
static constexpr int NSPLIT = 4;

#define LDS_AS __attribute__((address_space(3)))
#define GLB_AS __attribute__((address_space(1)))

__device__ __forceinline__ float bf2f(u16 v) { return __uint_as_float(((unsigned)v) << 16); }
__device__ __forceinline__ u16 f2bf(float f) {
  __hip_bfloat16 b = __float2bfloat16(f);
  return *reinterpret_cast<u16*>(&b);
}
__device__ __forceinline__ float rbf(float f) { return bf2f(f2bf(f)); }
__device__ __forceinline__ unsigned pack2(float lo, float hi) {
  return (unsigned)f2bf(lo) | ((unsigned)f2bf(hi) << 16);
}

__device__ __forceinline__ void glds16(const void* g, void* l) {
  __builtin_amdgcn_global_load_lds((const GLB_AS unsigned*)g, (LDS_AS unsigned*)l, 16, 0, 0);
}
__device__ __forceinline__ bf16x4 tr_read(const char* p) {
  return __builtin_amdgcn_ds_read_tr16_b64_v4i16((LDS_AS bf16x4*)p);
}
__device__ __forceinline__ bf16x8 cat8(bf16x4 a, bf16x4 b) {
  return __builtin_shufflevector(a, b, 0, 1, 2, 3, 4, 5, 6, 7);
}
#define WAIT_V(n) asm volatile("s_waitcnt vmcnt(" #n ")" ::: "memory")
#define BAR() do { asm volatile("s_waitcnt lgkmcnt(0)" ::: "memory"); __builtin_amdgcn_s_barrier(); asm volatile("" ::: "memory"); } while (0)

static constexpr int CV_NB = 48;
static constexpr int CV_KV_BYTES = 8 * CV_NB * 16;
static constexpr int CV_CS8 = 552;
static constexpr int CV_HC_BYTES = 4 * CV_CS8 * 8;

__global__ __launch_bounds__(256) void conv_kernel(const u16* __restrict__ kin, const u16* __restrict__ vin,
                                                   const u16* __restrict__ shift_k, const u16* __restrict__ ssm_k,
                                                   const u16* __restrict__ ssm_D, u16* __restrict__ yout) {
  __shared__ __attribute__((aligned(16))) char lds[4 * CV_KV_BYTES + 4 * CV_HC_BYTES];
  char* kvs = lds;
  char* hcs = lds + 4 * CV_KV_BYTES;
  const int tid = threadIdx.x, lane = tid & 63, wave = tid >> 6;
  const int d0 = blockIdx.x * 4;

  const int t0 = tid * 8;
  float sk[4][4], dd[4];
#pragma unroll
  for (int c = 0; c < 4; ++c) {
#pragma unroll
    for (int j = 0; j < 4; ++j) sk[c][j] = bf2f(shift_k[(d0 + c) * 4 + j]);
    dd[c] = bf2f(ssm_D[d0 + c]);
  }
  u32x2 kr[11], vr[8];
#pragma unroll
  for (int i = 0; i < 11; ++i) {
    int t = t0 - 3 + i;
    if (t >= 0) kr[i] = *(const u32x2*)(kin + (size_t)t * D_ + d0);
    else kr[i] = (u32x2){0u, 0u};
  }
#pragma unroll
  for (int i = 0; i < 8; ++i) vr[i] = *(const u32x2*)(vin + (size_t)(t0 + i) * D_ + d0);

  float kvf[4][8];
#pragma unroll
  for (int c = 0; c < 4; ++c) {
#pragma unroll
    for (int i = 0; i < 8; ++i) {
      float acc = 0.f;
#pragma unroll
      for (int j = 0; j < 4; ++j) {
        unsigned w = kr[i + j][c >> 1];
        float kvv = bf2f((u16)((c & 1) ? (w >> 16) : (w & 0xffff)));
        acc = rbf(acc + rbf(kvv * sk[c][3 - j]));
      }
      unsigned w = vr[i][c >> 1];
      float vv = bf2f((u16)((c & 1) ? (w >> 16) : (w & 0xffff)));
      kvf[c][i] = rbf(acc * vv);
    }
  }
  {
    const int plane = (t0 & 63) >> 3, blk = (t0 >> 6) + 16;
#pragma unroll
    for (int c = 0; c < 4; ++c) {
      u32x4 w;
      w[0] = pack2(kvf[c][7], kvf[c][6]);
      w[1] = pack2(kvf[c][5], kvf[c][4]);
      w[2] = pack2(kvf[c][3], kvf[c][2]);
      w[3] = pack2(kvf[c][1], kvf[c][0]);
      *(u32x4*)(kvs + c * CV_KV_BYTES + (plane * CV_NB + blk) * 16) = w;
    }
    for (int i = tid; i < 512; i += 256) {
      int c = i >> 7, pl = (i >> 4) & 7, b = i & 15;
      *(u32x4*)(kvs + c * CV_KV_BYTES + (pl * CV_NB + b) * 16) = (u32x4){0u, 0u, 0u, 0u};
    }
  }
  for (int it = tid; it < 4 * 264; it += 256) {
    int c = it / 264, qp = it % 264;
    int q = qp - 4;
    const u16* hp = ssm_k + (size_t)(d0 + c) * S_;
    u32x4 a = (u32x4){0u, 0u, 0u, 0u}, b = (u32x4){0u, 0u, 0u, 0u};
    if (q >= 0 && q < 256) a = *(const u32x4*)(hp + q * 8);
    if (q + 1 >= 0 && q + 1 < 256) b = *(const u32x4*)(hp + (q + 1) * 8);
    char* dst = hcs + c * CV_HC_BYTES + qp * 16;
    *(u32x4*)(dst + 0 * CV_CS8 * 8) = a;
    u32x4 s1;
    s1[0] = __builtin_amdgcn_alignbit(a[1], a[0], 16);
    s1[1] = __builtin_amdgcn_alignbit(a[2], a[1], 16);
    s1[2] = __builtin_amdgcn_alignbit(a[3], a[2], 16);
    s1[3] = __builtin_amdgcn_alignbit(b[0], a[3], 16);
    *(u32x4*)(dst + 1 * CV_CS8 * 8) = s1;
    u32x4 s2 = (u32x4){a[1], a[2], a[3], b[0]};
    *(u32x4*)(dst + 2 * CV_CS8 * 8) = s2;
    u32x4 s3;
    s3[0] = s1[1]; s3[1] = s1[2]; s3[2] = s1[3];
    s3[3] = __builtin_amdgcn_alignbit(b[1], b[0], 16);
    *(u32x4*)(dst + 3 * CV_CS8 * 8) = s3;
  }
  __syncthreads();

  const int c16 = lane & 15, g = lane >> 4;
  f32x4 acc[4][2];
#pragma unroll
  for (int i = 0; i < 4; ++i) { acc[i][0] = (f32x4){0, 0, 0, 0}; acc[i][1] = (f32x4){0, 0, 0, 0}; }
  {
    const char* kvb = kvs + wave * CV_KV_BYTES + (g * CV_NB + 16 + c16) * 16;
    const int r = (c16 + 1) & 3, w = (c16 + 1) >> 2;
    const char* hb = hcs + wave * CV_HC_BYTES + (r * CV_CS8 - 2 * g + 6 + w) * 8;
    bf16x8 Gm2 = (bf16x8){0, 0, 0, 0, 0, 0, 0, 0}, Gm1 = Gm2;
    for (int dl = 0; dl < 32; ++dl) {
      bf16x8 G[4];
#pragma unroll
      for (int i = 0; i < 4; ++i) {
        const char* p = hb + (4 * dl + i) * 32;
        bf16x4 lo = *(const bf16x4*)p;
        bf16x4 hi = *(const bf16x4*)(p + 8);
        G[i] = cat8(lo, hi);
      }
      bf16x8 B1[2];
#pragma unroll
      for (int j = 0; j < 2; ++j) B1[j] = *(const bf16x8*)(kvb + j * 4 * CV_NB * 16 + (16 - dl) * 16);
#pragma unroll
      for (int i = 0; i < 4; ++i) acc[i][1] = __builtin_amdgcn_mfma_f32_16x16x32_bf16(G[i], B1[0], acc[i][1], 0, 0, 0);
      acc[0][1] = __builtin_amdgcn_mfma_f32_16x16x32_bf16(Gm2, B1[1], acc[0][1], 0, 0, 0);
      acc[1][1] = __builtin_amdgcn_mfma_f32_16x16x32_bf16(Gm1, B1[1], acc[1][1], 0, 0, 0);
      acc[2][1] = __builtin_amdgcn_mfma_f32_16x16x32_bf16(G[0], B1[1], acc[2][1], 0, 0, 0);
      acc[3][1] = __builtin_amdgcn_mfma_f32_16x16x32_bf16(G[1], B1[1], acc[3][1], 0, 0, 0);
      if (dl < 16) {
        bf16x8 B0[2];
#pragma unroll
        for (int j = 0; j < 2; ++j) B0[j] = *(const bf16x8*)(kvb + j * 4 * CV_NB * 16 + (0 - dl) * 16);
#pragma unroll
        for (int i = 0; i < 4; ++i) acc[i][0] = __builtin_amdgcn_mfma_f32_16x16x32_bf16(G[i], B0[0], acc[i][0], 0, 0, 0);
        acc[0][0] = __builtin_amdgcn_mfma_f32_16x16x32_bf16(Gm2, B0[1], acc[0][0], 0, 0, 0);
        acc[1][0] = __builtin_amdgcn_mfma_f32_16x16x32_bf16(Gm1, B0[1], acc[1][0], 0, 0, 0);
        acc[2][0] = __builtin_amdgcn_mfma_f32_16x16x32_bf16(G[0], B0[1], acc[2][0], 0, 0, 0);
        acc[3][0] = __builtin_amdgcn_mfma_f32_16x16x32_bf16(G[1], B0[1], acc[3][0], 0, 0, 0);
      }
      Gm2 = G[2]; Gm1 = G[3];
    }
  }
  __syncthreads();
  {
    char* yt = hcs + wave * 4096;
#pragma unroll
    for (int ct = 0; ct < 2; ++ct)
#pragma unroll
      for (int i = 0; i < 4; ++i) {
        int t = 64 * (16 * ct + c16) + 16 * i + 4 * g;
        u32x2 w;
        w[0] = pack2(acc[i][ct][0], acc[i][ct][1]);
        w[1] = pack2(acc[i][ct][2], acc[i][ct][3]);
        *(u32x2*)(yt + t * 2) = w;
      }
  }
  __syncthreads();
  {
    u32x4 yc[4];
#pragma unroll
    for (int c = 0; c < 4; ++c) yc[c] = *(const u32x4*)(hcs + c * 4096 + t0 * 2);
#pragma unroll
    for (int i = 0; i < 8; ++i) {
      float o[4];
#pragma unroll
      for (int c = 0; c < 4; ++c) {
        unsigned w = yc[c][i >> 1];
        float yv = bf2f((u16)((i & 1) ? (w >> 16) : (w & 0xffff)));
        o[c] = yv + rbf(kvf[c][i] * dd[c]);
      }
      u32x2 w;
      w[0] = pack2(o[0], o[1]);
      w[1] = pack2(o[2], o[3]);
      *(u32x2*)(yout + (size_t)(t0 + i) * D_ + d0) = w;
    }
  }
}

__device__ __forceinline__ float sum16(float v) {
  v += __shfl_xor(v, 1, 16);
  v += __shfl_xor(v, 2, 16);
  v += __shfl_xor(v, 4, 16);
  v += __shfl_xor(v, 8, 16);
  return v;
}
__device__ __forceinline__ void layernorm36(const float (&x)[36], float (&o)[36]) {
  float s = 0.f;
#pragma unroll
  for (int i = 0; i < 36; ++i) s += x[i];
  s = sum16(s);
  const float mean = s * (1.0f / D_);
  float q = 0.f;
#pragma unroll
  for (int i = 0; i < 36; ++i) { float d = x[i] - mean; q += d * d; }
  q = sum16(q);
  const float var = q * (1.0f / D_);
  const float mb = rbf(mean);
  const float vb = rbf(var);
  const float sd = rbf(sqrtf(rbf(vb + 1e-5f)));
#pragma unroll
  for (int i = 0; i < 36; ++i) o[i] = rbf(rbf(x[i] - mb) / sd);
}

template <int MODE>
__global__ __launch_bounds__(256) void row_kernel(const u16* __restrict__ xin, const float* __restrict__ part,
                                                  const u16* __restrict__ y, u16* __restrict__ xout,
                                                  u16* __restrict__ xn_t) {
  const int tid = threadIdx.x;
  const int l16 = tid & 15;
  const int row = blockIdx.x * 16 + (tid >> 4);
  float x[36];
#pragma unroll
  for (int i = 0; i < 9; ++i) {
    u32x2 w = *(const u32x2*)(xin + (size_t)row * D_ + (l16 + 16 * i) * 4);
    x[4 * i + 0] = bf2f((u16)(w[0] & 0xffff)); x[4 * i + 1] = bf2f((u16)(w[0] >> 16));
    x[4 * i + 2] = bf2f((u16)(w[1] & 0xffff)); x[4 * i + 3] = bf2f((u16)(w[1] >> 16));
  }
  if (MODE >= 1) {
#pragma unroll
    for (int i = 0; i < 9; ++i) {
      f32x4 a = (f32x4){0, 0, 0, 0};
#pragma unroll
      for (int s = 0; s < NSPLIT; ++s)
        a += *(const f32x4*)(part + ((size_t)s * S_ + row) * D_ + (l16 + 16 * i) * 4);
#pragma unroll
      for (int e = 0; e < 4; ++e) x[4 * i + e] = rbf(x[4 * i + e] + rbf(a[e]));
    }
  }
  if (MODE <= 1) {
    float n[36];
    layernorm36(x, n);
#pragma unroll
    for (int i = 0; i < 9; ++i) {
      u32x2 w = *(const u32x2*)(y + (size_t)row * D_ + (l16 + 16 * i) * 4);
      float y0 = bf2f((u16)(w[0] & 0xffff)), y1 = bf2f((u16)(w[0] >> 16));
      float y2 = bf2f((u16)(w[1] & 0xffff)), y3 = bf2f((u16)(w[1] >> 16));
      x[4 * i + 0] = rbf(x[4 * i + 0] + rbf(n[4 * i + 0] * y0));
      x[4 * i + 1] = rbf(x[4 * i + 1] + rbf(n[4 * i + 1] * y1));
      x[4 * i + 2] = rbf(x[4 * i + 2] + rbf(n[4 * i + 2] * y2));
      x[4 * i + 3] = rbf(x[4 * i + 3] + rbf(n[4 * i + 3] * y3));
    }
  }
#pragma unroll
  for (int i = 0; i < 9; ++i) {
    u32x2 w;
    w[0] = pack2(x[4 * i + 0], x[4 * i + 1]);
    w[1] = pack2(x[4 * i + 2], x[4 * i + 3]);
    *(u32x2*)(xout + (size_t)row * D_ + (l16 + 16 * i) * 4) = w;
  }
  if (MODE <= 1) {
    float n[36];
    layernorm36(x, n);
    const int tt = row >> 4, c = row & 15;
#pragma unroll
    for (int i = 0; i < 9; ++i) {
      int k = (l16 + 16 * i) * 4;
      int kt = k >> 5, kk = k & 31, half = kk >> 4, g = (kk & 15) >> 2;
      u32x2 w;
      w[0] = pack2(n[4 * i + 0], n[4 * i + 1]);
      w[1] = pack2(n[4 * i + 2], n[4 * i + 3]);
      *(u32x2*)((char*)xn_t + ((size_t)(tt * KT1 + kt) * 64 + g * 16 + c) * 16 + half * 8) = w;
    }
  }
}

static constexpr int STG = 34816;
static constexpr int NSTG = 4;

__device__ __forceinline__ float silu_mul(float g, float u) {
  float gb = rbf(g), ub = rbf(u);
  float s = rbf(gb / (1.0f + __expf(-gb)));
  return s * ub;
}

__global__ __launch_bounds__(512, 2) void gemm1_kernel(const u16* __restrict__ xn_t, const u16* __restrict__ w1,
                                                       const u16* __restrict__ w3, u16* __restrict__ h_t) {
  __shared__ __attribute__((aligned(16))) char lds[NSTG * STG];
  const int tid = threadIdx.x, lane = tid & 63;
  const int wave = __builtin_amdgcn_readfirstlane(tid >> 6);
  const int wm = wave & 3, wn = wave >> 2;
  const int bid = blockIdx.x, xcd = bid & 7, idx = bid >> 3;
  const int ft = xcd * 4 + (idx & 3), mt = idx >> 2;

  const char* xs = (const char*)xn_t + ((size_t)(mt * 16 + 2 * wave) * KT1) * 1024 + lane * 16;
  const char *ws0, *ws1, *ws2;
  {
    int ci = wave * 128 + lane;
    int img = ci / 576, rem = ci % 576, row = rem / 18, cc = rem % 18;
    ws0 = (const char*)((img ? w3 : w1) + (size_t)row * F_ + ft * 144 + cc * 8);
    ci += 64;
    img = ci / 576; rem = ci % 576; row = rem / 18; cc = rem % 18;
    ws1 = (const char*)((img ? w3 : w1) + (size_t)row * F_ + ft * 144 + cc * 8);
    ci = 1024 + wave * 16 + (lane & 15);
    img = ci / 576; rem = ci % 576; row = rem / 18; cc = rem % 18;
    ws2 = (const char*)((img ? w3 : w1) + (size_t)row * F_ + ft * 144 + cc * 8);
  }
  constexpr size_t WSTEP = (size_t)32 * F_ * 2;
  auto issue = [&](int s) {
    char* sb = lds + (s & 3) * STG;
    glds16(xs + s * 1024, sb + (2 * wave) * 1024);
    glds16(xs + KT1 * 1024 + s * 1024, sb + (2 * wave + 1) * 1024);
    glds16(ws0 + s * WSTEP, sb + 16384 + wave * 2048);
    glds16(ws1 + s * WSTEP, sb + 16384 + wave * 2048 + 1024);
    if (lane < 16) glds16(ws2 + s * WSTEP, sb + 16384 + 16384 + wave * 256);
  };

  f32x4 acc[9][4];
#pragma unroll
  for (int n = 0; n < 9; ++n)
#pragma unroll
    for (int m = 0; m < 4; ++m) acc[n][m] = (f32x4){0, 0, 0, 0};

  const int g = lane >> 4, q = (lane & 15) >> 2, p = lane & 3;
  const int cb = wn ? 80 : 0, cm = 64 + 8 * wn;
  const int xoff = wm * 4096 + lane * 16;
  const int woff = 16384 + (4 * g + q) * 288 + (cb + 4 * p) * 2;
  const int moff = 16384 + (p >> 1) * 9216 + (4 * g + q) * 288 + (cm + 4 * (p & 1)) * 2;

  issue(0); issue(1); issue(2);
  for (int s = 0; s < KT1; ++s) {
    if (s < KT1 - 2) WAIT_V(10); else if (s == KT1 - 2) WAIT_V(5); else WAIT_V(0);
    BAR();
    if (s + 3 < KT1) issue(s + 3);
    const char* sb = lds + (s & 3) * STG;
    bf16x8 xf[4];
#pragma unroll
    for (int m = 0; m < 4; ++m) xf[m] = *(const bf16x8*)(sb + xoff + m * 1024);
#pragma unroll
    for (int n = 0; n < 9; ++n) {
      const char* wp = (n < 8) ? (sb + woff + (n >> 2) * 9216 + (n & 3) * 32) : (sb + moff);
      bf16x8 wf = cat8(tr_read(wp), tr_read(wp + 16 * 288));
#pragma unroll
      for (int m = 0; m < 4; ++m) acc[n][m] = __builtin_amdgcn_mfma_f32_16x16x32_bf16(wf, xf[m], acc[n][m], 0, 0, 0);
    }
  }

  const int c = lane & 15;
  const int Tbase = ft * 9 + (wn ? 5 : 0);
#pragma unroll
  for (int m = 0; m < 4; ++m) {
    const int tt = mt * 16 + wm * 4 + m;
    char* hb = (char*)h_t + (size_t)tt * KT2 * 1024 + lane * 16;
#pragma unroll
    for (int n = 0; n < 4; ++n) {
      int T = Tbase + n;
      u32x2 w;
      w[0] = pack2(silu_mul(acc[n][m][0], acc[4 + n][m][0]), silu_mul(acc[n][m][1], acc[4 + n][m][1]));
      w[1] = pack2(silu_mul(acc[n][m][2], acc[4 + n][m][2]), silu_mul(acc[n][m][3], acc[4 + n][m][3]));
      *(u32x2*)(hb + (size_t)(T >> 1) * 1024 + (T & 1) * 8) = w;
    }
    {
      f32x4 o;
#pragma unroll
      for (int r = 0; r < 4; ++r) o[r] = __shfl_xor(acc[8][m][r], 32, 64);
      if (lane < 32) {
        int T = ft * 9 + 4;
        u32x2 w;
        w[0] = pack2(silu_mul(acc[8][m][0], o[0]), silu_mul(acc[8][m][1], o[1]));
        w[1] = pack2(silu_mul(acc[8][m][2], o[2]), silu_mul(acc[8][m][3], o[3]));
        int gs = 2 * wn + g;
        *(u32x2*)((char*)h_t + (size_t)tt * KT2 * 1024 + (size_t)(T >> 1) * 1024 + (gs * 16 + c) * 16 + (T & 1) * 8) = w;
      }
    }
  }
}

__global__ __launch_bounds__(512, 2) void gemm2_kernel(const u16* __restrict__ h_t, const u16* __restrict__ w2,
                                                       float* __restrict__ part) {
  __shared__ __attribute__((aligned(16))) char lds[NSTG * STG];
  const int tid = threadIdx.x, lane = tid & 63;
  const int wave = __builtin_amdgcn_readfirstlane(tid >> 6);
  const int wm = wave & 3, wk = wave >> 2;
  const int bid = blockIdx.x, xcd = bid & 7, idx = bid >> 3;
  const int ks = xcd >> 1;
  const int nt = idx & 3, mt = (xcd & 1) * 8 + (idx >> 2);
  const int kt0 = ks * 36;

  const char* xs = (const char*)h_t + ((size_t)(mt * 8 + wm * 2) * KT2 + kt0 + wk * 18) * 1024 + lane * 16;
  const char *ws0, *ws1, *ws2;
  {
    int ci = wave * 128 + lane;
    int img = ci / 576, rem = ci % 576, row = rem / 18, cc = rem % 18;
    ws0 = (const char*)(w2 + (size_t)((kt0 + img * 18) * 32 + row) * D_ + nt * 144 + cc * 8);
    ci += 64;
    img = ci / 576; rem = ci % 576; row = rem / 18; cc = rem % 18;
    ws1 = (const char*)(w2 + (size_t)((kt0 + img * 18) * 32 + row) * D_ + nt * 144 + cc * 8);
    ci = 1024 + wave * 16 + (lane & 15);
    img = ci / 576; rem = ci % 576; row = rem / 18; cc = rem % 18;
    ws2 = (const char*)(w2 + (size_t)((kt0 + img * 18) * 32 + row) * D_ + nt * 144 + cc * 8);
  }
  constexpr size_t WSTEP = (size_t)32 * D_ * 2;
  auto issue = [&](int s) {
    char* sb = lds + (s & 3) * STG;
    glds16(xs + s * 1024, sb + (2 * wave) * 1024);
    glds16(xs + (size_t)KT2 * 1024 + s * 1024, sb + (2 * wave + 1) * 1024);
    glds16(ws0 + s * WSTEP, sb + 16384 + wave * 2048);
    glds16(ws1 + s * WSTEP, sb + 16384 + wave * 2048 + 1024);
    if (lane < 16) glds16(ws2 + s * WSTEP, sb + 16384 + 16384 + wave * 256);
  };

  f32x4 acc[9][2];
#pragma unroll
  for (int n = 0; n < 9; ++n) { acc[n][0] = (f32x4){0, 0, 0, 0}; acc[n][1] = (f32x4){0, 0, 0, 0}; }

  const int g = lane >> 4, q = (lane & 15) >> 2, p = lane & 3;
  const int xoff = wave * 2048 + lane * 16;
  const int woff = 16384 + wk * 9216 + (4 * g + q) * 288 + (4 * p) * 2;

  issue(0); issue(1); issue(2);
  for (int s = 0; s < 18; ++s) {
    if (s < 16) WAIT_V(10); else if (s == 16) WAIT_V(5); else WAIT_V(0);
    BAR();
    if (s + 3 < 18) issue(s + 3);
    const char* sb = lds + (s & 3) * STG;
    bf16x8 xf[2];
#pragma unroll
    for (int m = 0; m < 2; ++m) xf[m] = *(const bf16x8*)(sb + xoff + m * 1024);
#pragma unroll
    for (int n = 0; n < 9; ++n) {
      const char* wp = sb + woff + n * 32;
      bf16x8 wf = cat8(tr_read(wp), tr_read(wp + 16 * 288));
#pragma unroll
      for (int m = 0; m < 2; ++m) acc[n][m] = __builtin_amdgcn_mfma_f32_16x16x32_bf16(wf, xf[m], acc[n][m], 0, 0, 0);
    }
  }
  BAR();
  f32x4* red = (f32x4*)lds;
  if (wk == 1) {
#pragma unroll
    for (int n = 0; n < 9; ++n)
#pragma unroll
      for (int m = 0; m < 2; ++m) red[(wm * 18 + n * 2 + m) * 64 + lane] = acc[n][m];
  }
  BAR();
  if (wk == 0) {
    const int c = lane & 15;
#pragma unroll
    for (int m = 0; m < 2; ++m) {
      const int token = mt * 128 + wm * 32 + m * 16 + c;
      float* dst = part + ((size_t)ks * S_ + token) * D_ + nt * 144 + 4 * g;
#pragma unroll
      for (int n = 0; n < 9; ++n) {
        f32x4 o = acc[n][m] + red[(wm * 18 + n * 2 + m) * 64 + lane];
        *(f32x4*)(dst + n * 16) = o;
      }
    }
  }
}

extern "C" void kernel_launch(void* const* d_in, const int* in_sizes, int n_in,
                              void* d_out, int out_size, void* d_ws, size_t ws_size,
                              hipStream_t stream) {
  const u16* A_q = (const u16*)d_in[0];
  const u16* A_k = (const u16*)d_in[1];
  const u16* A_v = (const u16*)d_in[2];
  const u16* A_shift = (const u16*)d_in[3];
  const u16* A_ssmk = (const u16*)d_in[4];
  const u16* A_ssmD = (const u16*)d_in[5];
  const u16* B_w1 = (const u16*)d_in[7];
  const u16* B_w3 = (const u16*)d_in[8];
  const u16* B_w2 = (const u16*)d_in[9];
  u16* out = (u16*)d_out;

  char* ws = (char*)d_ws;
  constexpr size_t XB = (size_t)S_ * D_ * 2;
  u16* y = (u16*)(ws);
  u16* xa = (u16*)(ws + XB);
  u16* xb = (u16*)(ws + 2 * XB);
  u16* xn = (u16*)(ws + 3 * XB);
  u16* h = (u16*)(ws + 4 * XB);
  float* part = (float*)(ws + 4 * XB + (size_t)S_ * F_ * 2);

  conv_kernel<<<D_ / 4, 256, 0, stream>>>(A_k, A_v, A_shift, A_ssmk, A_ssmD, y);
  row_kernel<0><<<S_ / 16, 256, 0, stream>>>(A_q, nullptr, y, xa, xn);
  gemm1_kernel<<<256, 512, 0, stream>>>(xn, B_w1, B_w3, h);
  gemm2_kernel<<<256, 512, 0, stream>>>(h, B_w2, part);
  row_kernel<1><<<S_ / 16, 256, 0, stream>>>(xa, part, y, xb, xn);
  gemm1_kernel<<<256, 512, 0, stream>>>(xn, B_w1, B_w3, h);
  gemm2_kernel<<<256, 512, 0, stream>>>(h, B_w2, part);
  row_kernel<2><<<S_ / 16, 256, 0, stream>>>(xb, part, nullptr, out, nullptr);
}
```

```cpp
#include <hip/hip_runtime.h>
#include <hip/hip_bf16.h>
#include <stdint.h>

typedef __attribute__((ext_vector_type(8))) short bf16x8;
typedef __attribute__((ext_vector_type(4))) short bf16x4;
typedef __attribute__((ext_vector_type(4))) float f32x4;
typedef __attribute__((ext_vector_type(4))) unsigned u32x4;
typedef __attribute__((ext_vector_type(2))) unsigned u32x2;
typedef unsigned short u16;

static constexpr int S_ = 2048, D_ = 576, F_ = 4608;
static constexpr int KT1 = D_ / 32;
static constexpr int KT2 = F_ / 32;
static constexpr int NSPLIT = 4;

#define LDS_AS __attribute__((address_space(3)))
#define GLB_AS __attribute__((address_space(1)))

__device__ __forceinline__ float bf2f(u16 v) { return __uint_as_float(((unsigned)v) << 16); }
__device__ __forceinline__ u16 f2bf(float f) {
  __hip_bfloat16 b = __float2bfloat16(f);
  return *reinterpret_cast<u16*>(&b);
}
__device__ __forceinline__ float rbf(float f) { return bf2f(f2bf(f)); }
__device__ __forceinline__ unsigned pack2(float lo, float hi) {
  return (unsigned)f2bf(lo) | ((unsigned)f2bf(hi) << 16);
}

typedef __attribute__((ext_vector_type(2))) float f32x2;
typedef __attribute__((ext_vector_type(2))) __bf16 xbf16x2;
__device__ __forceinline__ unsigned cvt2(float lo, float hi) {
  f32x2 v = {lo, hi};
  xbf16x2 b = __builtin_convertvector(v, xbf16x2);
  return __builtin_bit_cast(unsigned, b);
}
__device__ __forceinline__ float lo16(unsigned w) { return __uint_as_float(w << 16); }
__device__ __forceinline__ float hi16(unsigned w) { return __uint_as_float(w & 0xffff0000u); }
__device__ __forceinline__ unsigned lds_addr(const void* l) { return (unsigned)(size_t)(LDS_AS const char*)l; }
__device__ __forceinline__ void st8(void* p, u32x2 v) {
  *(u32x2*)p = v;
}
__device__ __forceinline__ void st16(void* p, f32x4 v) {
  *(f32x4*)p = v;
}
__device__ __forceinline__ void glds16(const void* g, unsigned l) {
  unsigned keep;
  l = __builtin_amdgcn_readfirstlane(l);
  asm volatile("s_mov_b32 %0, m0\n\ts_mov_b32 m0, %2\n\ts_nop 0\n\tglobal_load_lds_dwordx4 %1, off\n\ts_mov_b32 m0, %0"
               : "=&s"(keep) : "v"(g), "s"(l) : "memory");
}
__device__ __forceinline__ bf16x4 tr_read(const char* p) {
  return __builtin_amdgcn_ds_read_tr16_b64_v4i16((LDS_AS bf16x4*)p);
}
__device__ __forceinline__ bf16x8 cat8(bf16x4 a, bf16x4 b) {
  return __builtin_shufflevector(a, b, 0, 1, 2, 3, 4, 5, 6, 7);
}
#define WAIT_V(n) asm volatile("s_waitcnt vmcnt(" #n ")" ::: "memory")
#define BAR() do { asm volatile("s_waitcnt lgkmcnt(0)" ::: "memory"); __builtin_amdgcn_s_barrier(); asm volatile("" ::: "memory"); } while (0)

static constexpr int CV_NB = 48;
static constexpr int CV_KV_BYTES = 8 * CV_NB * 16;
static constexpr int CV_CS8 = 552;
static constexpr int CV_HC_BYTES = 4 * CV_CS8 * 8;

struct ConvFr { bf16x8 G[4]; bf16x8 B[2]; };
__device__ __forceinline__ void conv_load(ConvFr& f, const char* hb, const char* kvb, int dl) {
#pragma unroll
  for (int i = 0; i < 4; ++i) {
    const char* p = hb + (4 * dl + i) * 32;
    f.G[i] = cat8(*(const bf16x4*)p, *(const bf16x4*)(p + 8));
  }
#pragma unroll
  for (int j = 0; j < 2; ++j) f.B[j] = *(const bf16x8*)(kvb + j * 4 * CV_NB * 16 - dl * 16);
}
__device__ __forceinline__ void conv_mma(const ConvFr& f, bf16x8& Gm2, bf16x8& Gm1, f32x4 (&acc)[4]) {
#pragma unroll
  for (int i = 0; i < 4; ++i) acc[i] = __builtin_amdgcn_mfma_f32_16x16x32_bf16(f.G[i], f.B[0], acc[i], 0, 0, 0);
  acc[0] = __builtin_amdgcn_mfma_f32_16x16x32_bf16(Gm2, f.B[1], acc[0], 0, 0, 0);
  acc[1] = __builtin_amdgcn_mfma_f32_16x16x32_bf16(Gm1, f.B[1], acc[1], 0, 0, 0);
  acc[2] = __builtin_amdgcn_mfma_f32_16x16x32_bf16(f.G[0], f.B[1], acc[2], 0, 0, 0);
  acc[3] = __builtin_amdgcn_mfma_f32_16x16x32_bf16(f.G[1], f.B[1], acc[3], 0, 0, 0);
  Gm2 = f.G[2]; Gm1 = f.G[3];
}
__device__ __forceinline__ void conv_part(const char* hb, const char* kvb, int d0, int d1, f32x4 (&acc)[4]) {
  bf16x8 Gm2, Gm1;
  if (d0 == 0) { Gm2 = (bf16x8){0, 0, 0, 0, 0, 0, 0, 0}; Gm1 = Gm2; }
  else {
    const char* p = hb + (4 * d0 - 2) * 32;
    Gm2 = cat8(*(const bf16x4*)p, *(const bf16x4*)(p + 8));
    Gm1 = cat8(*(const bf16x4*)(p + 32), *(const bf16x4*)(p + 40));
  }
  ConvFr fa, fb;
  conv_load(fa, hb, kvb, d0);
#pragma unroll 1
  for (int dl = d0; dl < d1; dl += 2) {
    conv_load(fb, hb, kvb, dl + 1);
    conv_mma(fa, Gm2, Gm1, acc);
    if (dl + 2 < d1) conv_load(fa, hb, kvb, dl + 2);
    conv_mma(fb, Gm2, Gm1, acc);
  }
}

__global__ __launch_bounds__(512) void conv_kernel(const u16* __restrict__ kin, const u16* __restrict__ vin,
                                                   const u16* __restrict__ shift_k, const u16* __restrict__ ssm_k,
                                                   const u16* __restrict__ ssm_D, u16* __restrict__ yout) {
  __shared__ __attribute__((aligned(16))) char lds[4 * CV_KV_BYTES + 4 * CV_HC_BYTES + 16384];
  char* kvs = lds;
  char* hcs = lds + 4 * CV_KV_BYTES;
  char* red = lds + 4 * CV_KV_BYTES + 4 * CV_HC_BYTES;
  const int tid = threadIdx.x, lane = tid & 63, wave = tid >> 6;
  const int d0 = blockIdx.x * 4;

  const int t0 = tid * 4;
  u32x2 kr[7], vr[4];
#pragma unroll
  for (int i = 0; i < 7; ++i) {
    int t = t0 - 3 + i;
    if (t >= 0) kr[i] = *(const u32x2*)(kin + (size_t)t * D_ + d0);
    else kr[i] = (u32x2){0u, 0u};
  }
#pragma unroll
  for (int i = 0; i < 4; ++i) vr[i] = *(const u32x2*)(vin + (size_t)(t0 + i) * D_ + d0);
  for (int it = tid; it < 4 * 264; it += 512) {
    int c = it / 264, qp = it % 264;
    int q = qp - 4;
    const u16* hp = ssm_k + (size_t)(d0 + c) * S_;
    u32x4 a = (u32x4){0u, 0u, 0u, 0u}, b = (u32x4){0u, 0u, 0u, 0u};
    if (q >= 0 && q < 256) a = *(const u32x4*)(hp + q * 8);
    if (q + 1 >= 0 && q + 1 < 256) b = *(const u32x4*)(hp + (q + 1) * 8);
    char* dst = hcs + c * CV_HC_BYTES + qp * 16;
    *(u32x4*)(dst + 0 * CV_CS8 * 8) = a;
    u32x4 s1;
    s1[0] = __builtin_amdgcn_alignbit(a[1], a[0], 16);
    s1[1] = __builtin_amdgcn_alignbit(a[2], a[1], 16);
    s1[2] = __builtin_amdgcn_alignbit(a[3], a[2], 16);
    s1[3] = __builtin_amdgcn_alignbit(b[0], a[3], 16);
    *(u32x4*)(dst + 1 * CV_CS8 * 8) = s1;
    u32x4 s2 = (u32x4){a[1], a[2], a[3], b[0]};
    *(u32x4*)(dst + 2 * CV_CS8 * 8) = s2;
    u32x4 s3;
    s3[0] = s1[1]; s3[1] = s1[2]; s3[2] = s1[3];
    s3[3] = __builtin_amdgcn_alignbit(b[1], b[0], 16);
    *(u32x4*)(dst + 3 * CV_CS8 * 8) = s3;
  }
  {
    int c = tid >> 7, pl = (tid >> 4) & 7, b = tid & 15;
    *(u32x4*)(kvs + c * CV_KV_BYTES + (pl * CV_NB + b) * 16) = (u32x4){0u, 0u, 0u, 0u};
  }
  float sk[4][4], dd[4];
#pragma unroll
  for (int c = 0; c < 4; ++c) {
#pragma unroll
    for (int j = 0; j < 4; ++j) sk[c][j] = bf2f(shift_k[(d0 + c) * 4 + j]);
    dd[c] = bf2f(ssm_D[d0 + c]);
  }
  float kvf[4][4];
#pragma unroll
  for (int c = 0; c < 4; ++c) {
#pragma unroll
    for (int i = 0; i < 4; ++i) {
      float acc = 0.f;
#pragma unroll
      for (int j = 0; j < 4; ++j) {
        unsigned w = kr[i + j][c >> 1];
        float kvv = bf2f((u16)((c & 1) ? (w >> 16) : (w & 0xffff)));
        acc = rbf(acc + rbf(kvv * sk[c][3 - j]));
      }
      unsigned w = vr[i][c >> 1];
      float vv = bf2f((u16)((c & 1) ? (w >> 16) : (w & 0xffff)));
      kvf[c][i] = rbf(acc * vv);
    }
  }
  {
    const int plane = (t0 & 63) >> 3, blk = (t0 >> 6) + 16, hi = (t0 >> 2) & 1;
#pragma unroll
    for (int c = 0; c < 4; ++c) {
      u32x2 w;
      w[0] = pack2(kvf[c][3], kvf[c][2]);
      w[1] = pack2(kvf[c][1], kvf[c][0]);
      *(u32x2*)(kvs + c * CV_KV_BYTES + (plane * CV_NB + blk) * 16 + (hi ? 0 : 8)) = w;
    }
  }
  __syncthreads();

  const int ch = wave & 3, part = wave >> 2;
  const int c16 = lane & 15, g = lane >> 4;
  f32x4 accA[4], accB[4];
#pragma unroll
  for (int i = 0; i < 4; ++i) { accA[i] = (f32x4){0, 0, 0, 0}; accB[i] = (f32x4){0, 0, 0, 0}; }
  {
    const char* kvb = kvs + ch * CV_KV_BYTES + (g * CV_NB + 16 + c16) * 16;
    const int r = (c16 + 1) & 3, w = (c16 + 1) >> 2;
    const char* hb = hcs + ch * CV_HC_BYTES + (r * CV_CS8 - 2 * g + 6 + w) * 8;
    if (part == 0) {
      conv_part(hb, kvb + 16 * 16, 0, 24, accA);
    } else {
      conv_part(hb, kvb, 0, 16, accA);
      conv_part(hb, kvb + 16 * 16, 24, 32, accB);
#pragma unroll
      for (int i = 0; i < 4; ++i) *(f32x4*)(red + ((ch * 4 + i) * 64 + lane) * 16) = accB[i];
    }
  }
  __syncthreads();
  {
    char* yt = hcs + ch * 4096;
    const int ct = part ? 0 : 1;
#pragma unroll
    for (int i = 0; i < 4; ++i) {
      f32x4 a = accA[i];
      if (part == 0) a += *(const f32x4*)(red + ((ch * 4 + i) * 64 + lane) * 16);
      int t = 64 * (16 * ct + c16) + 16 * i + 4 * g;
      u32x2 w;
      w[0] = pack2(a[0], a[1]);
      w[1] = pack2(a[2], a[3]);
      *(u32x2*)(yt + t * 2) = w;
    }
  }
  __syncthreads();
  {
    u32x2 yc[4];
#pragma unroll
    for (int c = 0; c < 4; ++c) yc[c] = *(const u32x2*)(hcs + c * 4096 + t0 * 2);
#pragma unroll
    for (int i = 0; i < 4; ++i) {
      float o[4];
#pragma unroll
      for (int c = 0; c < 4; ++c) {
        unsigned w = yc[c][i >> 1];
        float yv = bf2f((u16)((i & 1) ? (w >> 16) : (w & 0xffff)));
        o[c] = yv + rbf(kvf[c][i] * dd[c]);
      }
      u32x2 w;
      w[0] = pack2(o[0], o[1]);
      w[1] = pack2(o[2], o[3]);
      *(u32x2*)(yout + (size_t)(t0 + i) * D_ + d0) = w;
    }
  }
}

__device__ __forceinline__ float sum64(float v) {
#pragma unroll
  for (int o = 1; o < 64; o <<= 1) v += __shfl_xor(v, o, 64);
  return v;
}
__device__ __forceinline__ void layernorm9(const float (&x)[9], float (&o)[9]) {
  float s = 0.f;
#pragma unroll
  for (int i = 0; i < 9; ++i) s += x[i];
  s = sum64(s);
  const float mean = s * (1.0f / D_);
  float q = 0.f;
#pragma unroll
  for (int i = 0; i < 9; ++i) { float d = x[i] - mean; q += d * d; }
  q = sum64(q);
  const float var = q * (1.0f / D_);
  const float mb = rbf(mean);
  const float vb = rbf(var);
  const float sd = rbf(sqrtf(rbf(vb + 1e-5f)));
#pragma unroll
  for (int i = 0; i < 9; ++i) o[i] = rbf(rbf(x[i] - mb) / sd);
}
__device__ __forceinline__ void load9(const u16* __restrict__ p, int l, float (&x)[9]) {
  u32x2 a = *(const u32x2*)(p + 4 * l), b = *(const u32x2*)(p + 256 + 4 * l);
  u16 c = p[512 + l];
  x[0] = lo16(a[0]); x[1] = hi16(a[0]); x[2] = lo16(a[1]); x[3] = hi16(a[1]);
  x[4] = lo16(b[0]); x[5] = hi16(b[0]); x[6] = lo16(b[1]); x[7] = hi16(b[1]);
  x[8] = bf2f(c);
}

template <int MODE>
__global__ __launch_bounds__(256) void row_kernel(const u16* __restrict__ xin, const float* __restrict__ part,
                                                  const u16* __restrict__ y, u16* __restrict__ xout,
                                                  u16* __restrict__ xn_t) {
  const int l = threadIdx.x & 63;
  const int row = blockIdx.x * 4 + (threadIdx.x >> 6);
  float x[9], yv[9];
  load9(xin + (size_t)row * D_, l, x);
  if (MODE <= 1) load9(y + (size_t)row * D_, l, yv);
  if (MODE >= 1) {
    f32x4 a = (f32x4){0, 0, 0, 0}, b = (f32x4){0, 0, 0, 0};
    float c = 0.f;
#pragma unroll
    for (int s = 0; s < NSPLIT; ++s) {
      const float* pp = part + ((size_t)s * S_ + row) * D_;
      a += *(const f32x4*)(pp + 4 * l);
      b += *(const f32x4*)(pp + 256 + 4 * l);
      c += pp[512 + l];
    }
#pragma unroll
    for (int e = 0; e < 4; ++e) { x[e] = rbf(x[e] + rbf(a[e])); x[4 + e] = rbf(x[4 + e] + rbf(b[e])); }
    x[8] = rbf(x[8] + rbf(c));
  }
  if (MODE <= 1) {
    float n[9];
    layernorm9(x, n);
#pragma unroll
    for (int i = 0; i < 9; ++i) x[i] = rbf(x[i] + rbf(n[i] * yv[i]));
  }
  {
    u16* po = xout + (size_t)row * D_;
    u32x2 w;
    w[0] = cvt2(x[0], x[1]); w[1] = cvt2(x[2], x[3]);
    *(u32x2*)(po + 4 * l) = w;
    w[0] = cvt2(x[4], x[5]); w[1] = cvt2(x[6], x[7]);
    *(u32x2*)(po + 256 + 4 * l) = w;
    po[512 + l] = f2bf(x[8]);
  }
  if (MODE <= 1) {
    float n[9];
    layernorm9(x, n);
    const int tt = row >> 4, c = row & 15;
    char* base = (char*)xn_t + (size_t)tt * KT1 * 1024 + c * 16;
    u32x2 w;
    w[0] = cvt2(n[0], n[1]); w[1] = cvt2(n[2], n[3]);
    *(u32x2*)(base + (l >> 3) * 1024 + (l & 3) * 256 + ((l >> 2) & 1) * 8) = w;
    w[0] = cvt2(n[4], n[5]); w[1] = cvt2(n[6], n[7]);
    *(u32x2*)(base + (8 + (l >> 3)) * 1024 + (l & 3) * 256 + ((l >> 2) & 1) * 8) = w;
    *(u16*)(base + (16 + (l >> 5)) * 1024 + ((l >> 2) & 3) * 256 + ((l >> 4) & 1) * 8 + (l & 3) * 2) = f2bf(n[8]);
  }
}

static constexpr int STG = 34816;
static constexpr int NSTG = 4;

__device__ __forceinline__ u32x2 silu_mul4(f32x4 g, f32x4 u) {
  u32x2 o;
#pragma unroll
  for (int i = 0; i < 2; ++i) {
    unsigned gw = cvt2(g[2 * i], g[2 * i + 1]);
    unsigned uw = cvt2(u[2 * i], u[2 * i + 1]);
    float g0 = lo16(gw), g1 = hi16(gw);
    float s0 = g0 * __builtin_amdgcn_rcpf(1.0f + __builtin_amdgcn_exp2f(g0 * -1.44269504089f));
    float s1 = g1 * __builtin_amdgcn_rcpf(1.0f + __builtin_amdgcn_exp2f(g1 * -1.44269504089f));
    unsigned sw = cvt2(s0, s1);
    o[i] = cvt2(lo16(sw) * lo16(uw), hi16(sw) * hi16(uw));
  }
  return o;
}

template <int N> __device__ __forceinline__ void wait_vm2(bf16x8& a, bf16x8& b) {
  asm volatile("s_waitcnt vmcnt(%2)" : "+v"(a), "+v"(b) : "i"(N) : "memory");
}
__device__ __forceinline__ void wait_vm2_dyn(int n, bf16x8& a, bf16x8& b) {
  switch (n) {
    case 0: wait_vm2<0>(a, b); break;
    case 1: wait_vm2<1>(a, b); break;
    case 2: wait_vm2<2>(a, b); break;
    case 3: wait_vm2<3>(a, b); break;
    case 4: wait_vm2<4>(a, b); break;
    case 5: wait_vm2<5>(a, b); break;
    case 6: wait_vm2<6>(a, b); break;
    case 7: wait_vm2<7>(a, b); break;
    case 8: wait_vm2<8>(a, b); break;
    case 9: wait_vm2<9>(a, b); break;
    case 10: wait_vm2<10>(a, b); break;
    case 11: wait_vm2<11>(a, b); break;
    case 12: wait_vm2<12>(a, b); break;
    case 13: wait_vm2<13>(a, b); break;
    case 14: wait_vm2<14>(a, b); break;
    case 15: wait_vm2<15>(a, b); break;
    case 16: wait_vm2<16>(a, b); break;
    case 17: wait_vm2<17>(a, b); break;
    case 18: wait_vm2<18>(a, b); break;
    case 19: wait_vm2<19>(a, b); break;
    case 20: wait_vm2<20>(a, b); break;
    case 21: wait_vm2<21>(a, b); break;
    case 22: wait_vm2<22>(a, b); break;
    case 23: wait_vm2<23>(a, b); break;
    case 24: wait_vm2<24>(a, b); break;
    case 25: wait_vm2<25>(a, b); break;
    case 26: wait_vm2<26>(a, b); break;
    case 27: wait_vm2<27>(a, b); break;
    case 28: wait_vm2<28>(a, b); break;
    case 29: wait_vm2<29>(a, b); break;
    case 30: wait_vm2<30>(a, b); break;
    case 31: wait_vm2<31>(a, b); break;
    default: wait_vm2<0>(a, b); break;
  }
}
template <int N> __device__ __forceinline__ void wait_vm() {
  asm volatile("s_waitcnt vmcnt(%0)" :: "i"(N) : "memory");
}
__device__ __forceinline__ bf16x8 gload16(const void* p) {
  bf16x8 r;
  asm volatile("global_load_dwordx4 %0, %1, off" : "=v"(r) : "v"(p) : "memory");
  return r;
}
__device__ __forceinline__ u32x2 swiglu4(f32x4 g, f32x4 u) {
  f32x4 e;
#pragma unroll
  for (int i = 0; i < 4; ++i) e[i] = __builtin_amdgcn_exp2f(g[i] * -1.44269504089f);
  e = e + 1.0f;
#pragma unroll
  for (int i = 0; i < 4; ++i) e[i] = __builtin_amdgcn_rcpf(e[i]);
  f32x4 h = (g * u) * e;
  u32x2 o;
  o[0] = cvt2(h[0], h[1]);
  o[1] = cvt2(h[2], h[3]);
  return o;
}

__global__ __launch_bounds__(512, 2) void gemm1_kernel(const u16* __restrict__ xn_t, const u16* __restrict__ w1,
                                                       const u16* __restrict__ w3, u16* __restrict__ h_t) {
  __shared__ __attribute__((aligned(16))) char lds[NSTG * STG];
  const int tid = threadIdx.x, lane = tid & 63;
  const int wave = __builtin_amdgcn_readfirstlane(tid >> 6);
  const int wm = wave & 3, wn = wave >> 2;
  const int bid = blockIdx.x, xcd = bid & 7, idx = bid >> 3;
  const int ft = xcd * 4 + (idx & 3), mt = idx >> 2;
  const char* xs = (const char*)xn_t + ((size_t)(mt * 16 + 2 * wave) * KT1) * 1024 + lane * 16;
  const char *ws0, *ws1, *ws2;
  {
    int ci = wave * 128 + lane;
    int img = ci / 576, rem = ci % 576, row = rem / 18, cc = rem % 18;
    ws0 = (const char*)((img ? w3 : w1) + (size_t)row * F_ + ft * 144 + cc * 8);
    ci += 64;
    img = ci / 576; rem = ci % 576; row = rem / 18; cc = rem % 18;
    ws1 = (const char*)((img ? w3 : w1) + (size_t)row * F_ + ft * 144 + cc * 8);
    ci = 1024 + wave * 16 + (lane & 15);
    img = ci / 576; rem = ci % 576; row = rem / 18; cc = rem % 18;
    ws2 = (const char*)((img ? w3 : w1) + (size_t)row * F_ + ft * 144 + cc * 8);
  }
  constexpr size_t WSTEP = (size_t)32 * F_ * 2;
  const unsigned lbase = lds_addr(lds);
  auto issue = [&](int s) {
    unsigned sb = lbase + (s & 3) * STG;
    glds16(xs + s * 1024, sb + (2 * wave) * 1024);
    glds16(xs + KT1 * 1024 + s * 1024, sb + (2 * wave + 1) * 1024);
    glds16(ws0 + s * WSTEP, sb + 16384 + wave * 2048);
    glds16(ws1 + s * WSTEP, sb + 16384 + wave * 2048 + 1024);
    if (lane < 16) glds16(ws2 + s * WSTEP, sb + 16384 + 16384 + wave * 256);
  };

  f32x4 acc[9][4];
#pragma unroll
  for (int n = 0; n < 9; ++n)
#pragma unroll
    for (int m = 0; m < 4; ++m) acc[n][m] = (f32x4){0, 0, 0, 0};

  const int g = lane >> 4, q = (lane & 15) >> 2, p = lane & 3;
  const int cb = wn ? 80 : 0, cm = 64 + 8 * wn;
  const int xoff = wm * 4096 + lane * 16;
  const int woff = 16384 + (4 * g + q) * 288 + (cb + 4 * p) * 2;
  const int moff = 16384 + (p >> 1) * 9216 + (4 * g + q) * 288 + (cm + 4 * (p & 1)) * 2;

  issue(0); issue(1); issue(2);
  auto wread = [&](const char* sb, int n) -> bf16x8 {
    const char* wp = (n < 8) ? (sb + woff + (n >> 2) * 9216 + (n & 3) * 32) : (sb + moff);
    return cat8(tr_read(wp), tr_read(wp + 16 * 288));
  };
  bf16x8 xa[4], xb[4], fa, fb, fc;
  WAIT_V(10);
  __builtin_amdgcn_s_barrier();
  asm volatile("" ::: "memory");
#pragma unroll
  for (int m = 0; m < 4; ++m) xa[m] = *(const bf16x8*)(lds + xoff + m * 1024);
  fa = wread(lds, 0);
  fb = wread(lds, 1);

#define SB0 __builtin_amdgcn_sched_barrier(0)
#define MM(n, w, x) _Pragma("unroll") for (int m = 0; m < 4; ++m) acc[n][m] = __builtin_amdgcn_mfma_f32_16x16x32_bf16(w, x[m], acc[n][m], 0, 0, 0)
  auto body = [&](int s, bf16x8 (&xc)[4], bf16x8 (&xn)[4], bool last) {
    const char* sb = lds + (s & 3) * STG;
    const char* sn = lds + ((s + 1) & 3) * STG;
    fc = wread(sb, 2); SB0;
    MM(0, fa, xc); SB0; fa = wread(sb, 3); SB0;
    MM(1, fb, xc); SB0; fb = wread(sb, 4); SB0;
    MM(2, fc, xc); SB0; fc = wread(sb, 5); SB0;
    MM(3, fa, xc); SB0; fa = wread(sb, 6); SB0;
    MM(4, fb, xc); SB0; fb = wread(sb, 7); SB0;
    MM(5, fc, xc); SB0; fc = wread(sb, 8); SB0;
    if (!last) {
      if (s < KT1 - 2) WAIT_V(5); else WAIT_V(0);
      __builtin_amdgcn_s_barrier();
      asm volatile("" ::: "memory");
#pragma unroll
      for (int m = 0; m < 4; ++m) xn[m] = *(const bf16x8*)(sn + xoff + m * 1024);
      SB0;
    }
    MM(6, fa, xc); SB0; if (!last) fa = wread(sn, 0); SB0;
    if (!last) { if (s + 3 < KT1) issue(s + 3); }
    SB0;
    MM(7, fb, xc); SB0; if (!last) fb = wread(sn, 1); SB0;
    MM(8, fc, xc); SB0;
  };
  for (int s = 0; s < KT1 - 2; s += 2) {
    body(s, xa, xb, false);
    body(s + 1, xb, xa, false);
  }
  body(KT1 - 2, xa, xb, false);
  body(KT1 - 1, xb, xa, true);
#undef MM

  const int c = lane & 15;
  const int Tbase = ft * 9 + (wn ? 5 : 0);
#pragma unroll
  for (int m = 0; m < 4; ++m) {
    const int tt = mt * 16 + wm * 4 + m;
    char* hb = (char*)h_t + (size_t)tt * KT2 * 1024 + lane * 16;
#pragma unroll
    for (int n = 0; n < 4; ++n) {
      int T = Tbase + n;
      u32x2 w = swiglu4(acc[n][m], acc[4 + n][m]);
      st8(hb + (size_t)(T >> 1) * 1024 + (T & 1) * 8, w);
    }
    {
      f32x4 o;
#pragma unroll
      for (int r = 0; r < 4; ++r) o[r] = __shfl_xor(acc[8][m][r], 32, 64);
      if (lane < 32) {
        int T = ft * 9 + 4;
        u32x2 w = swiglu4(acc[8][m], o);
        int gs = 2 * wn + g;
        st8((char*)h_t + (size_t)tt * KT2 * 1024 + (size_t)(T >> 1) * 1024 + (gs * 16 + c) * 16 + (T & 1) * 8, w);
      }
    }
  }
}

__global__ __launch_bounds__(512, 2) void gemm2_kernel(const u16* __restrict__ h_t, const u16* __restrict__ w2,
                                                       float* __restrict__ part) {
  __shared__ __attribute__((aligned(16))) char lds[NSTG * STG];
  const int tid = threadIdx.x, lane = tid & 63;
  const int wave = __builtin_amdgcn_readfirstlane(tid >> 6);
  const int wm = wave & 3, wk = wave >> 2;
  const int bid = blockIdx.x, xcd = bid & 7, idx = bid >> 3;
  const int ks = xcd >> 1;
  const int nt = idx & 3, mt = (xcd & 1) * 8 + (idx >> 2);
  const int kt0 = ks * 36;

  const char* xs = (const char*)h_t + ((size_t)(mt * 8 + wm * 2) * KT2 + kt0 + wk * 18) * 1024 + lane * 16;
  const char *ws0, *ws1, *ws2;
  {
    int ci = wave * 128 + lane;
    int img = ci / 576, rem = ci % 576, row = rem / 18, cc = rem % 18;
    ws0 = (const char*)(w2 + (size_t)((kt0 + img * 18) * 32 + row) * D_ + nt * 144 + cc * 8);
    ci += 64;
    img = ci / 576; rem = ci % 576; row = rem / 18; cc = rem % 18;
    ws1 = (const char*)(w2 + (size_t)((kt0 + img * 18) * 32 + row) * D_ + nt * 144 + cc * 8);
    ci = 1024 + wave * 16 + (lane & 15);
    img = ci / 576; rem = ci % 576; row = rem / 18; cc = rem % 18;
    ws2 = (const char*)(w2 + (size_t)((kt0 + img * 18) * 32 + row) * D_ + nt * 144 + cc * 8);
  }
  constexpr size_t WSTEP = (size_t)32 * D_ * 2;
  const unsigned lbase = lds_addr(lds);
  auto issue = [&](int s) {
    unsigned sb = lbase + (s & 3) * STG;
    glds16(xs + s * 1024, sb + (2 * wave) * 1024);
    glds16(xs + (size_t)KT2 * 1024 + s * 1024, sb + (2 * wave + 1) * 1024);
    glds16(ws0 + s * WSTEP, sb + 16384 + wave * 2048);
    glds16(ws1 + s * WSTEP, sb + 16384 + wave * 2048 + 1024);
    if (lane < 16) glds16(ws2 + s * WSTEP, sb + 16384 + 16384 + wave * 256);
  };

  f32x4 acc[9][2];
#pragma unroll
  for (int n = 0; n < 9; ++n) { acc[n][0] = (f32x4){0, 0, 0, 0}; acc[n][1] = (f32x4){0, 0, 0, 0}; }

  const int g = lane >> 4, q = (lane & 15) >> 2, p = lane & 3;
  const int xoff = wave * 2048 + lane * 16;
  const int woff = 16384 + wk * 9216 + (4 * g + q) * 288 + (4 * p) * 2;

  issue(0); issue(1); issue(2);
  WAIT_V(10);
  __builtin_amdgcn_s_barrier();
  if (wk == 1) __builtin_amdgcn_s_barrier();
  asm volatile("" ::: "memory");
  for (int s = 0; s < 18; ++s) {
    if (s + 3 < 18) issue(s + 3);
    const char* sb = lds + (s & 3) * STG;
    bf16x8 xf[2], wf[9];
#pragma unroll
    for (int m = 0; m < 2; ++m) xf[m] = *(const bf16x8*)(sb + xoff + m * 1024);
#pragma unroll
    for (int n = 0; n < 9; ++n) {
      const char* wp = sb + woff + n * 32;
      wf[n] = cat8(tr_read(wp), tr_read(wp + 16 * 288));
    }
    if (wk == 1) { if (s < 15) WAIT_V(10); else if (s == 15) WAIT_V(5); else WAIT_V(0); }
    asm volatile("s_waitcnt lgkmcnt(0)" ::: "memory");
    __builtin_amdgcn_sched_barrier(0);
    __builtin_amdgcn_s_barrier();
    __builtin_amdgcn_sched_barrier(0);
    __builtin_amdgcn_s_setprio(1);
#pragma unroll
    for (int n = 0; n < 9; ++n)
#pragma unroll
      for (int m = 0; m < 2; ++m) acc[n][m] = __builtin_amdgcn_mfma_f32_16x16x32_bf16(wf[n], xf[m], acc[n][m], 0, 0, 0);
    __builtin_amdgcn_s_setprio(0);
    if (wk == 0) { if (s < 15) WAIT_V(10); else if (s == 15) WAIT_V(5); else WAIT_V(0); }
    __builtin_amdgcn_sched_barrier(0);
    __builtin_amdgcn_s_barrier();
    __builtin_amdgcn_sched_barrier(0);
    asm volatile("" ::: "memory");
  }
  if (wk == 0) __builtin_amdgcn_s_barrier();
  BAR();
  f32x4* red = (f32x4*)lds;
  if (wk == 1) {
#pragma unroll
    for (int n = 0; n < 9; ++n)
#pragma unroll
      for (int m = 0; m < 2; ++m) red[(wm * 18 + n * 2 + m) * 64 + lane] = acc[n][m];
  }
  BAR();
  if (wk == 0) {
    const int c = lane & 15;
#pragma unroll
    for (int m = 0; m < 2; ++m) {
      const int token = mt * 128 + wm * 32 + m * 16 + c;
      float* dst = part + ((size_t)ks * S_ + token) * D_ + nt * 144 + 4 * g;
#pragma unroll
      for (int n = 0; n < 9; ++n) {
        f32x4 o = acc[n][m] + red[(wm * 18 + n * 2 + m) * 64 + lane];
        st16(dst + n * 16, o);
      }
    }
  }
}

extern "C" void kernel_launch(void* const* d_in, const int* in_sizes, int n_in,
                              void* d_out, int out_size, void* d_ws, size_t ws_size,
                              hipStream_t stream) {
  const u16* A_q = (const u16*)d_in[0];
  const u16* A_k = (const u16*)d_in[1];
  const u16* A_v = (const u16*)d_in[2];
  const u16* A_shift = (const u16*)d_in[3];
  const u16* A_ssmk = (const u16*)d_in[4];
  const u16* A_ssmD = (const u16*)d_in[5];
  const u16* B_w1 = (const u16*)d_in[7];
  const u16* B_w3 = (const u16*)d_in[8];
  const u16* B_w2 = (const u16*)d_in[9];
  u16* out = (u16*)d_out;

  char* ws = (char*)d_ws;
  constexpr size_t XB = (size_t)S_ * D_ * 2;
  u16* y = (u16*)(ws);
  u16* xa = (u16*)(ws + XB);
  u16* xb = (u16*)(ws + 2 * XB);
  u16* xn = (u16*)(ws + 3 * XB);
  u16* h = (u16*)(ws + 4 * XB);
  float* part = (float*)(ws + 4 * XB + (size_t)S_ * F_ * 2);

  conv_kernel<<<D_ / 4, 512, 0, stream>>>(A_k, A_v, A_shift, A_ssmk, A_ssmD, y);
  row_kernel<0><<<S_ / 4, 256, 0, stream>>>(A_q, nullptr, y, xa, xn);
  gemm1_kernel<<<256, 512, 0, stream>>>(xn, B_w1, B_w3, h);
  gemm2_kernel<<<256, 512, 0, stream>>>(h, B_w2, part);
  row_kernel<1><<<S_ / 4, 256, 0, stream>>>(xa, part, y, xb, xn);
  gemm1_kernel<<<256, 512, 0, stream>>>(xn, B_w1, B_w3, h);
  gemm2_kernel<<<256, 512, 0, stream>>>(h, B_w2, part);
  row_kernel<2><<<S_ / 4, 256, 0, stream>>>(xb, part, nullptr, out, nullptr);
}
```

```cpp
#include <hip/hip_runtime.h>
#include <hip/hip_bf16.h>
#include <stdint.h>
#define ST_SC1 1

typedef __attribute__((ext_vector_type(8))) short bf16x8;
typedef __attribute__((ext_vector_type(4))) short bf16x4;
typedef __attribute__((ext_vector_type(4))) float f32x4;
typedef __attribute__((ext_vector_type(4))) unsigned u32x4;
typedef __attribute__((ext_vector_type(2))) unsigned u32x2;
typedef unsigned short u16;

static constexpr int S_ = 2048, D_ = 576, F_ = 4608;
static constexpr int KT1 = D_ / 32;
static constexpr int KT2 = F_ / 32;
static constexpr int NSPLIT = 4;

#define LDS_AS __attribute__((address_space(3)))
#define GLB_AS __attribute__((address_space(1)))

__device__ __forceinline__ float bf2f(u16 v) { return __uint_as_float(((unsigned)v) << 16); }
__device__ __forceinline__ u16 f2bf(float f) {
  __hip_bfloat16 b = __float2bfloat16(f);
  return *reinterpret_cast<u16*>(&b);
}
__device__ __forceinline__ float rbf(float f) { return bf2f(f2bf(f)); }
__device__ __forceinline__ unsigned pack2(float lo, float hi) {
  return (unsigned)f2bf(lo) | ((unsigned)f2bf(hi) << 16);
}

typedef __attribute__((ext_vector_type(2))) float f32x2;
typedef __attribute__((ext_vector_type(2))) __bf16 xbf16x2;
__device__ __forceinline__ unsigned cvt2(float lo, float hi) {
  f32x2 v = {lo, hi};
  xbf16x2 b = __builtin_convertvector(v, xbf16x2);
  return __builtin_bit_cast(unsigned, b);
}
__device__ __forceinline__ float lo16(unsigned w) { return __uint_as_float(w << 16); }
__device__ __forceinline__ float hi16(unsigned w) { return __uint_as_float(w & 0xffff0000u); }
__device__ __forceinline__ unsigned lds_addr(const void* l) { return (unsigned)(size_t)(LDS_AS const char*)l; }
__device__ __forceinline__ void st8(void* p, u32x2 v) {
  asm volatile("global_store_dwordx2 %0, %1, off sc0 sc1" :: "v"(p), "v"(v) : "memory");
}
__device__ __forceinline__ void st16u(void* p, u32x4 v) {
  asm volatile("global_store_dwordx4 %0, %1, off sc0 sc1\n\ts_nop 1" :: "v"(p), "v"(v) : "memory");
}
__device__ __forceinline__ void st16(void* p, f32x4 v) {
  asm volatile("global_store_dwordx4 %0, %1, off sc0 sc1\n\ts_nop 1" :: "v"(p), "v"(v) : "memory");
}
__device__ __forceinline__ void glds16(const void* g, unsigned l) {
  unsigned keep;
  l = __builtin_amdgcn_readfirstlane(l);
  asm volatile("s_mov_b32 %0, m0\n\ts_mov_b32 m0, %2\n\ts_nop 0\n\tglobal_load_lds_dwordx4 %1, off\n\ts_mov_b32 m0, %0"
               : "=&s"(keep) : "v"(g), "s"(l) : "memory");
}
__device__ __forceinline__ bf16x4 tr_read(const char* p) {
  return __builtin_amdgcn_ds_read_tr16_b64_v4i16((LDS_AS bf16x4*)p);
}
__device__ __forceinline__ bf16x8 cat8(bf16x4 a, bf16x4 b) {
  return __builtin_shufflevector(a, b, 0, 1, 2, 3, 4, 5, 6, 7);
}
#define KSTART(id)
#define KEND(id)
#define WAIT_V(n) asm volatile("s_waitcnt vmcnt(" #n ")" ::: "memory")
#define BAR() do { asm volatile("s_waitcnt lgkmcnt(0)" ::: "memory"); __builtin_amdgcn_s_barrier(); asm volatile("" ::: "memory"); } while (0)

static constexpr int CV_NB = 48;
static constexpr int CV_KV_BYTES = 8 * CV_NB * 16;
static constexpr int CV_CS8 = 552;
static constexpr int CV_HC_BYTES = 4 * CV_CS8 * 8;

__global__ __launch_bounds__(256) void kvt_kernel(const u16* __restrict__ kin, const u16* __restrict__ vin,
                                                  const u16* __restrict__ shift_k, u16* __restrict__ kvt) {
  __shared__ __attribute__((aligned(16))) u16 tl[64][72];
  const int tid = threadIdx.x;
  KSTART(0);
  const int blk = blockIdx.x & 31, cht = blockIdx.x >> 5;
  const int cg = tid & 7, c0 = cht * 64 + cg * 8;
  float sk[8][4];
#pragma unroll
  for (int e = 0; e < 8; ++e) {
    u32x2 w = *(const u32x2*)(shift_k + (c0 + e) * 4);
    sk[e][0] = lo16(w[0]); sk[e][1] = hi16(w[0]); sk[e][2] = lo16(w[1]); sk[e][3] = hi16(w[1]);
  }
#pragma unroll
  for (int ps = 0; ps < 2; ++ps) {
    const int r = (tid >> 3) + 32 * ps, t = blk * 64 + r;
    u32x4 kr[4];
#pragma unroll
    for (int j = 0; j < 4; ++j) {
      int tt = t - 3 + j;
      if (tt >= 0) kr[j] = *(const u32x4*)(kin + (size_t)tt * D_ + c0);
      else kr[j] = (u32x4){0u, 0u, 0u, 0u};
    }
    u32x4 vr = *(const u32x4*)(vin + (size_t)t * D_ + c0);
    const int slot = (r & ~7) + 7 - (r & 7);
#pragma unroll
    for (int e = 0; e < 8; ++e) {
      float acc = 0.f;
#pragma unroll
      for (int j = 0; j < 4; ++j) {
        float kvv = (e & 1) ? hi16(kr[j][e >> 1]) : lo16(kr[j][e >> 1]);
        acc = rbf(acc + rbf(kvv * sk[e][3 - j]));
      }
      float vv = (e & 1) ? hi16(vr[e >> 1]) : lo16(vr[e >> 1]);
      tl[cg * 8 + e][slot] = f2bf(acc * vv);
    }
  }
  __syncthreads();
#pragma unroll
  for (int ps = 0; ps < 2; ++ps) {
    const int ch = (tid >> 3) + 32 * ps, pl = tid & 7;
    u32x4 w = *(const u32x4*)(&tl[ch][pl * 8]);
    *(u32x4*)(kvt + ((size_t)(cht * 64 + ch) * 32 + blk) * 64 + pl * 8) = w;
  }
  KEND(0);
}

struct ConvFr { bf16x4 G[8]; bf16x8 B[2]; };
__device__ __forceinline__ void conv_load(ConvFr& f, unsigned ha, unsigned ka) {
  asm volatile("ds_read_b64 %0, %10\n\tds_read_b64 %1, %10 offset:8\n\t"
               "ds_read_b64 %2, %10 offset:32\n\tds_read_b64 %3, %10 offset:40\n\t"
               "ds_read_b64 %4, %10 offset:64\n\tds_read_b64 %5, %10 offset:72\n\t"
               "ds_read_b64 %6, %10 offset:96\n\tds_read_b64 %7, %10 offset:104\n\t"
               "ds_read_b128 %8, %11\n\tds_read_b128 %9, %11 offset:3072"
               : "=&v"(f.G[0]), "=&v"(f.G[1]), "=&v"(f.G[2]), "=&v"(f.G[3]), "=&v"(f.G[4]), "=&v"(f.G[5]), "=&v"(f.G[6]), "=&v"(f.G[7]),
                 "=&v"(f.B[0]), "=&v"(f.B[1])
               : "v"(ha), "v"(ka) : "memory");
}
template <int N> __device__ __forceinline__ void conv_wait(ConvFr& f) {
  asm volatile("s_waitcnt lgkmcnt(%10)"
               : "+v"(f.G[0]), "+v"(f.G[1]), "+v"(f.G[2]), "+v"(f.G[3]), "+v"(f.G[4]), "+v"(f.G[5]), "+v"(f.G[6]), "+v"(f.G[7]),
                 "+v"(f.B[0]), "+v"(f.B[1]) : "i"(N) : "memory");
}
__device__ __forceinline__ void conv_mma(const ConvFr& f, bf16x8& Gm2, bf16x8& Gm1, f32x4 (&acc)[4]) {
  bf16x8 G0 = cat8(f.G[0], f.G[1]), G1 = cat8(f.G[2], f.G[3]), G2 = cat8(f.G[4], f.G[5]), G3 = cat8(f.G[6], f.G[7]);
  acc[0] = __builtin_amdgcn_mfma_f32_16x16x32_bf16(G0, f.B[0], acc[0], 0, 0, 0);
  acc[1] = __builtin_amdgcn_mfma_f32_16x16x32_bf16(G1, f.B[0], acc[1], 0, 0, 0);
  acc[2] = __builtin_amdgcn_mfma_f32_16x16x32_bf16(G2, f.B[0], acc[2], 0, 0, 0);
  acc[3] = __builtin_amdgcn_mfma_f32_16x16x32_bf16(G3, f.B[0], acc[3], 0, 0, 0);
  acc[0] = __builtin_amdgcn_mfma_f32_16x16x32_bf16(Gm2, f.B[1], acc[0], 0, 0, 0);
  acc[1] = __builtin_amdgcn_mfma_f32_16x16x32_bf16(Gm1, f.B[1], acc[1], 0, 0, 0);
  acc[2] = __builtin_amdgcn_mfma_f32_16x16x32_bf16(G0, f.B[1], acc[2], 0, 0, 0);
  acc[3] = __builtin_amdgcn_mfma_f32_16x16x32_bf16(G1, f.B[1], acc[3], 0, 0, 0);
  Gm2 = G2; Gm1 = G3;
}
__device__ __forceinline__ void conv_part(const char* hb, const char* kvb, int d0, int d1, f32x4 (&acc)[4]) {
  bf16x8 Gm2, Gm1;
  if (d0 == 0) { Gm2 = (bf16x8){0, 0, 0, 0, 0, 0, 0, 0}; Gm1 = Gm2; }
  else {
    const char* p = hb + (4 * d0 - 2) * 32;
    Gm2 = cat8(*(const bf16x4*)p, *(const bf16x4*)(p + 8));
    Gm1 = cat8(*(const bf16x4*)(p + 32), *(const bf16x4*)(p + 40));
  }
  unsigned ha = lds_addr(hb) + d0 * 128, ka = lds_addr(kvb) - d0 * 16;
  ConvFr fa, fb;
  conv_load(fa, ha, ka);
#pragma unroll 1
  for (int dl = d0; dl < d1; dl += 2) {
    conv_load(fb, ha + 128, ka - 16);
    conv_wait<10>(fa);
    conv_mma(fa, Gm2, Gm1, acc);
    __builtin_amdgcn_sched_barrier(0);
    ha += 256; ka -= 32;
    conv_load(fa, ha, ka);
    conv_wait<10>(fb);
    conv_mma(fb, Gm2, Gm1, acc);
    __builtin_amdgcn_sched_barrier(0);
  }
  conv_wait<0>(fa);
}

__global__ __launch_bounds__(512) void conv_kernel(const u16* __restrict__ kvt, const u16* __restrict__ ssm_k,
                                                   const u16* __restrict__ ssm_D, u16* __restrict__ yout) {
  __shared__ __attribute__((aligned(16))) char lds[4 * CV_KV_BYTES + 4 * CV_HC_BYTES + 16384];
  char* kvs = lds;
  char* hcs = lds + 4 * CV_KV_BYTES;
  char* red = lds + 4 * CV_KV_BYTES + 4 * CV_HC_BYTES;
  const int tid = threadIdx.x, lane = tid & 63, wave = tid >> 6;
  const int d0 = blockIdx.x * 4;
  KSTART(1);
  const int t0 = tid * 4;
#pragma unroll
  for (int ps = 0; ps < 2; ++ps) {
    int it = tid + 512 * ps, c = it >> 8, blk = (it >> 3) & 31, pl = it & 7;
    u32x4 w = *(const u32x4*)(kvt + ((size_t)(d0 + c) * 32 + blk) * 64 + pl * 8);
    *(u32x4*)(kvs + c * CV_KV_BYTES + (pl * CV_NB + blk + 16) * 16) = w;
  }
  for (int it = tid; it < 4 * 264; it += 512) {
    int c = it / 264, qp = it % 264;
    int q = qp - 4;
    const u16* hp = ssm_k + (size_t)(d0 + c) * S_;
    u32x4 a = (u32x4){0u, 0u, 0u, 0u}, b = (u32x4){0u, 0u, 0u, 0u};
    if (q >= 0 && q < 256) a = *(const u32x4*)(hp + q * 8);
    if (q + 1 >= 0 && q + 1 < 256) b = *(const u32x4*)(hp + (q + 1) * 8);
    char* dst = hcs + c * CV_HC_BYTES + qp * 16;
    *(u32x4*)(dst + 0 * CV_CS8 * 8) = a;
    u32x4 s1;
    s1[0] = __builtin_amdgcn_alignbit(a[1], a[0], 16);
    s1[1] = __builtin_amdgcn_alignbit(a[2], a[1], 16);
    s1[2] = __builtin_amdgcn_alignbit(a[3], a[2], 16);
    s1[3] = __builtin_amdgcn_alignbit(b[0], a[3], 16);
    *(u32x4*)(dst + 1 * CV_CS8 * 8) = s1;
    u32x4 s2 = (u32x4){a[1], a[2], a[3], b[0]};
    *(u32x4*)(dst + 2 * CV_CS8 * 8) = s2;
    u32x4 s3;
    s3[0] = s1[1]; s3[1] = s1[2]; s3[2] = s1[3];
    s3[3] = __builtin_amdgcn_alignbit(b[1], b[0], 16);
    *(u32x4*)(dst + 3 * CV_CS8 * 8) = s3;
  }
  {
    int c = tid >> 7, pl = (tid >> 4) & 7, b = tid & 15;
    *(u32x4*)(kvs + c * CV_KV_BYTES + (pl * CV_NB + b) * 16) = (u32x4){0u, 0u, 0u, 0u};
  }
  float dd[4];
#pragma unroll
  for (int c = 0; c < 4; ++c) dd[c] = bf2f(ssm_D[d0 + c]);
  __syncthreads();

  const int ch = wave & 3, part = wave >> 2;
  const int c16 = lane & 15, g = lane >> 4;
  f32x4 accA[4], accB[4];
#pragma unroll
  for (int i = 0; i < 4; ++i) { accA[i] = (f32x4){0, 0, 0, 0}; accB[i] = (f32x4){0, 0, 0, 0}; }
  {
    const char* kvb = kvs + ch * CV_KV_BYTES + (g * CV_NB + 16 + c16) * 16;
    const int r = (c16 + 1) & 3, w = (c16 + 1) >> 2;
    const char* hb = hcs + ch * CV_HC_BYTES + (r * CV_CS8 - 2 * g + 6 + w) * 8;
    if (part == 0) {
      conv_part(hb, kvb + 16 * 16, 0, 24, accA);
    } else {
      conv_part(hb, kvb, 0, 16, accA);
      conv_part(hb, kvb + 16 * 16, 24, 32, accB);
#pragma unroll
      for (int i = 0; i < 4; ++i) *(f32x4*)(red + ((ch * 4 + i) * 64 + lane) * 16) = accB[i];
    }
  }
  __syncthreads();
  {
    char* yt = hcs + ch * 4096;
    const int ct = part ? 0 : 1;
#pragma unroll
    for (int i = 0; i < 4; ++i) {
      f32x4 a = accA[i];
      if (part == 0) a += *(const f32x4*)(red + ((ch * 4 + i) * 64 + lane) * 16);
      int t = 64 * (16 * ct + c16) + 16 * i + 4 * g;
      u32x2 w;
      w[0] = pack2(a[0], a[1]);
      w[1] = pack2(a[2], a[3]);
      *(u32x2*)(yt + t * 2) = w;
    }
  }
  __syncthreads();
  {
    u32x2 yc[4];
    float kvf[4][4];
    const int plane = (t0 & 63) >> 3, blk = (t0 >> 6) + 16, hi = (t0 >> 2) & 1;
#pragma unroll
    for (int c = 0; c < 4; ++c) {
      yc[c] = *(const u32x2*)(hcs + c * 4096 + t0 * 2);
      u32x2 kw = *(const u32x2*)(kvs + c * CV_KV_BYTES + (plane * CV_NB + blk) * 16 + (hi ? 0 : 8));
      kvf[c][3] = lo16(kw[0]); kvf[c][2] = hi16(kw[0]); kvf[c][1] = lo16(kw[1]); kvf[c][0] = hi16(kw[1]);
    }
#pragma unroll
    for (int i = 0; i < 4; ++i) {
      float o[4];
#pragma unroll
      for (int c = 0; c < 4; ++c) {
        unsigned w = yc[c][i >> 1];
        float yv = bf2f((u16)((i & 1) ? (w >> 16) : (w & 0xffff)));
        o[c] = yv + rbf(kvf[c][i] * dd[c]);
      }
      u32x2 w;
      w[0] = pack2(o[0], o[1]);
      w[1] = pack2(o[2], o[3]);
      *(u32x2*)(yout + (size_t)(t0 + i) * D_ + d0) = w;
    }
  }
  KEND(1);
}

__device__ __forceinline__ float sum64(float v) {
#pragma unroll
  for (int o = 1; o < 64; o <<= 1) v += __shfl_xor(v, o, 64);
  return v;
}
__device__ __forceinline__ void layernorm9(const float (&x)[9], float (&o)[9]) {
  float s = 0.f;
#pragma unroll
  for (int i = 0; i < 9; ++i) s += x[i];
  s = sum64(s);
  const float mean = s * (1.0f / D_);
  float q = 0.f;
#pragma unroll
  for (int i = 0; i < 9; ++i) { float d = x[i] - mean; q += d * d; }
  q = sum64(q);
  const float var = q * (1.0f / D_);
  const float mb = rbf(mean);
  const float vb = rbf(var);
  const float sd = rbf(sqrtf(rbf(vb + 1e-5f)));
#pragma unroll
  for (int i = 0; i < 9; ++i) o[i] = rbf(rbf(x[i] - mb) / sd);
}
__device__ __forceinline__ void load9(const u16* __restrict__ p, int l, float (&x)[9]) {
  u32x2 a = *(const u32x2*)(p + 4 * l), b = *(const u32x2*)(p + 256 + 4 * l);
  u16 c = p[512 + l];
  x[0] = lo16(a[0]); x[1] = hi16(a[0]); x[2] = lo16(a[1]); x[3] = hi16(a[1]);
  x[4] = lo16(b[0]); x[5] = hi16(b[0]); x[6] = lo16(b[1]); x[7] = hi16(b[1]);
  x[8] = bf2f(c);
}

template <int MODE, int KID>
__global__ __launch_bounds__(256) void row_kernel(const u16* __restrict__ xin, const float* __restrict__ part,
                                                  const u16* __restrict__ y, u16* __restrict__ xout,
                                                  u16* __restrict__ xn_t) {
  KSTART(KID);
  const int l = threadIdx.x & 63;
  const int row = blockIdx.x * 4 + (threadIdx.x >> 6);
  float x[9], yv[9];
  load9(xin + (size_t)row * D_, l, x);
  if (MODE <= 1) load9(y + (size_t)row * D_, l, yv);
  if (MODE >= 1) {
    f32x4 a = (f32x4){0, 0, 0, 0}, b = (f32x4){0, 0, 0, 0};
    float c = 0.f;
#pragma unroll
    for (int s = 0; s < NSPLIT; ++s) {
      const float* pp = part + ((size_t)s * S_ + row) * D_;
      a += *(const f32x4*)(pp + 4 * l);
      b += *(const f32x4*)(pp + 256 + 4 * l);
      c += pp[512 + l];
    }
#pragma unroll
    for (int e = 0; e < 4; ++e) { x[e] = rbf(x[e] + rbf(a[e])); x[4 + e] = rbf(x[4 + e] + rbf(b[e])); }
    x[8] = rbf(x[8] + rbf(c));
  }
  if (MODE <= 1) {
    float n[9];
    layernorm9(x, n);
#pragma unroll
    for (int i = 0; i < 9; ++i) x[i] = rbf(x[i] + rbf(n[i] * yv[i]));
  }
  {
    u16* po = xout + (size_t)row * D_;
    u32x2 w;
    w[0] = cvt2(x[0], x[1]); w[1] = cvt2(x[2], x[3]);
    *(u32x2*)(po + 4 * l) = w;
    w[0] = cvt2(x[4], x[5]); w[1] = cvt2(x[6], x[7]);
    *(u32x2*)(po + 256 + 4 * l) = w;
    po[512 + l] = f2bf(x[8]);
  }
  if (MODE <= 1) {
    float n[9];
    layernorm9(x, n);
    const int tt = row >> 4, c = row & 15;
    char* base = (char*)xn_t + (size_t)tt * KT1 * 1024 + c * 16;
    u32x2 w;
    w[0] = cvt2(n[0], n[1]); w[1] = cvt2(n[2], n[3]);
    *(u32x2*)(base + (l >> 3) * 1024 + (l & 3) * 256 + ((l >> 2) & 1) * 8) = w;
    w[0] = cvt2(n[4], n[5]); w[1] = cvt2(n[6], n[7]);
    *(u32x2*)(base + (8 + (l >> 3)) * 1024 + (l & 3) * 256 + ((l >> 2) & 1) * 8) = w;
    *(u16*)(base + (16 + (l >> 5)) * 1024 + ((l >> 2) & 3) * 256 + ((l >> 4) & 1) * 8 + (l & 3) * 2) = f2bf(n[8]);
  }
  KEND(KID);
}

static constexpr int STG = 34816;
static constexpr int NSTG = 4;

__device__ __forceinline__ u32x2 silu_mul4(f32x4 g, f32x4 u) {
  u32x2 o;
#pragma unroll
  for (int i = 0; i < 2; ++i) {
    unsigned gw = cvt2(g[2 * i], g[2 * i + 1]);
    unsigned uw = cvt2(u[2 * i], u[2 * i + 1]);
    float g0 = lo16(gw), g1 = hi16(gw);
    float s0 = g0 * __builtin_amdgcn_rcpf(1.0f + __builtin_amdgcn_exp2f(g0 * -1.44269504089f));
    float s1 = g1 * __builtin_amdgcn_rcpf(1.0f + __builtin_amdgcn_exp2f(g1 * -1.44269504089f));
    unsigned sw = cvt2(s0, s1);
    o[i] = cvt2(lo16(sw) * lo16(uw), hi16(sw) * hi16(uw));
  }
  return o;
}

template <int N> __device__ __forceinline__ void wait_vm2(bf16x8& a, bf16x8& b) {
  asm volatile("s_waitcnt vmcnt(%2)" : "+v"(a), "+v"(b) : "i"(N) : "memory");
}
__device__ __forceinline__ void wait_vm2_dyn(int n, bf16x8& a, bf16x8& b) {
  switch (n) {
    case 0: wait_vm2<0>(a, b); break;
    case 1: wait_vm2<1>(a, b); break;
    case 2: wait_vm2<2>(a, b); break;
    case 3: wait_vm2<3>(a, b); break;
    case 4: wait_vm2<4>(a, b); break;
    case 5: wait_vm2<5>(a, b); break;
    case 6: wait_vm2<6>(a, b); break;
    case 7: wait_vm2<7>(a, b); break;
    case 8: wait_vm2<8>(a, b); break;
    case 9: wait_vm2<9>(a, b); break;
    case 10: wait_vm2<10>(a, b); break;
    case 11: wait_vm2<11>(a, b); break;
    case 12: wait_vm2<12>(a, b); break;
    case 13: wait_vm2<13>(a, b); break;
    case 14: wait_vm2<14>(a, b); break;
    case 15: wait_vm2<15>(a, b); break;
    case 16: wait_vm2<16>(a, b); break;
    case 17: wait_vm2<17>(a, b); break;
    case 18: wait_vm2<18>(a, b); break;
    case 19: wait_vm2<19>(a, b); break;
    case 20: wait_vm2<20>(a, b); break;
    case 21: wait_vm2<21>(a, b); break;
    case 22: wait_vm2<22>(a, b); break;
    case 23: wait_vm2<23>(a, b); break;
    case 24: wait_vm2<24>(a, b); break;
    case 25: wait_vm2<25>(a, b); break;
    case 26: wait_vm2<26>(a, b); break;
    case 27: wait_vm2<27>(a, b); break;
    case 28: wait_vm2<28>(a, b); break;
    case 29: wait_vm2<29>(a, b); break;
    case 30: wait_vm2<30>(a, b); break;
    case 31: wait_vm2<31>(a, b); break;
    default: wait_vm2<0>(a, b); break;
  }
}
template <int N> __device__ __forceinline__ void wait_vm() {
  asm volatile("s_waitcnt vmcnt(%0)" :: "i"(N) : "memory");
}
__device__ __forceinline__ bf16x8 gload16(const void* p) {
  bf16x8 r;
  asm volatile("global_load_dwordx4 %0, %1, off" : "=v"(r) : "v"(p) : "memory");
  return r;
}
__device__ __forceinline__ u32x2 swiglu4(f32x4 g, f32x4 u) {
  f32x4 e;
#pragma unroll
  for (int i = 0; i < 4; ++i) e[i] = __builtin_amdgcn_exp2f(g[i] * -1.44269504089f);
  e = e + 1.0f;
#pragma unroll
  for (int i = 0; i < 4; ++i) e[i] = __builtin_amdgcn_rcpf(e[i]);
  f32x4 h = (g * u) * e;
  u32x2 o;
  o[0] = cvt2(h[0], h[1]);
  o[1] = cvt2(h[2], h[3]);
  return o;
}

template <int KID>
__global__ __launch_bounds__(512, 2) void gemm1_kernel(const u16* __restrict__ xn_t, const u16* __restrict__ w1,
                                                       const u16* __restrict__ w3, u16* __restrict__ h_t) {
  __shared__ __attribute__((aligned(16))) char lds[NSTG * STG];
  const int tid = threadIdx.x, lane = tid & 63;
  const int wave = __builtin_amdgcn_readfirstlane(tid >> 6);
  const int wm = wave & 3, wn = wave >> 2;
  const int bid = blockIdx.x, xcd = bid & 7, idx = bid >> 3;
  const int ft = xcd * 4 + (idx & 3), mt = idx >> 2;
  KSTART(KID);
  const char* xs = (const char*)xn_t + ((size_t)(mt * 16 + 2 * wave) * KT1) * 1024 + lane * 16;
  const char *ws0, *ws1, *ws2;
  {
    const int colA = 32 * (9 * (ft >> 1) + 4 * (ft & 1)), colB = 32 * (9 * (ft >> 1) + 8) + 16 * (ft & 1) - 128;
    int ci = wave * 128 + lane;
    int img = ci / 576, rem = ci % 576, row = rem / 18, cc = rem % 18;
    ws0 = (const char*)((img ? w3 : w1) + (size_t)row * F_ + (cc < 16 ? colA : colB) + cc * 8);
    ci += 64;
    img = ci / 576; rem = ci % 576; row = rem / 18; cc = rem % 18;
    ws1 = (const char*)((img ? w3 : w1) + (size_t)row * F_ + (cc < 16 ? colA : colB) + cc * 8);
    ci = 1024 + wave * 16 + (lane & 15);
    img = ci / 576; rem = ci % 576; row = rem / 18; cc = rem % 18;
    ws2 = (const char*)((img ? w3 : w1) + (size_t)row * F_ + (cc < 16 ? colA : colB) + cc * 8);
  }
  constexpr size_t WSTEP = (size_t)32 * F_ * 2;
  const unsigned lbase = lds_addr(lds);
  auto issue = [&](int s) {
    unsigned sb = lbase + (s & 3) * STG;
    glds16(xs + s * 1024, sb + (2 * wave) * 1024);
    glds16(xs + KT1 * 1024 + s * 1024, sb + (2 * wave + 1) * 1024);
    glds16(ws0 + s * WSTEP, sb + 16384 + wave * 2048);
    glds16(ws1 + s * WSTEP, sb + 16384 + wave * 2048 + 1024);
    if (lane < 16) glds16(ws2 + s * WSTEP, sb + 16384 + 16384 + wave * 256);
  };

  f32x4 acc[9][4];
#pragma unroll
  for (int n = 0; n < 9; ++n)
#pragma unroll
    for (int m = 0; m < 4; ++m) acc[n][m] = (f32x4){0, 0, 0, 0};

  const int g = lane >> 4, q = (lane & 15) >> 2, p = lane & 3;
  const int cb = wn * 64, cm = 128 + 8 * wn;
  const int xoff = wm * 4096 + lane * 16;
  const int woff = 16384 + (4 * g + q) * 288 + (cb + 4 * p) * 2;
  const int moff = 16384 + (p >> 1) * 9216 + (4 * g + q) * 288 + (cm + 4 * (p & 1)) * 2;

  issue(0); issue(1); issue(2);
  auto wread = [&](const char* sb, int n) -> bf16x8 {
    const char* wp = (n < 8) ? (sb + woff + (n >> 2) * 9216 + (n & 3) * 32) : (sb + moff);
    return cat8(tr_read(wp), tr_read(wp + 16 * 288));
  };
  bf16x8 xa[4], xb[4], fa, fb, fc;
  WAIT_V(10);
  __builtin_amdgcn_s_barrier();
  asm volatile("" ::: "memory");
#pragma unroll
  for (int m = 0; m < 4; ++m) xa[m] = *(const bf16x8*)(lds + xoff + m * 1024);
  fa = wread(lds, 0);
  fb = wread(lds, 1);

#define SB0 __builtin_amdgcn_sched_barrier(0)
#define MM(n, w, x) _Pragma("unroll") for (int m = 0; m < 4; ++m) acc[n][m] = __builtin_amdgcn_mfma_f32_16x16x32_bf16(w, x[m], acc[n][m], 0, 0, 0)
  auto body = [&](int s, bf16x8 (&xc)[4], bf16x8 (&xn)[4], bool last) {
    const char* sb = lds + (s & 3) * STG;
    const char* sn = lds + ((s + 1) & 3) * STG;
    fc = wread(sb, 2); SB0;
    MM(0, fa, xc); SB0; fa = wread(sb, 3); SB0;
    MM(1, fb, xc); SB0; fb = wread(sb, 4); SB0;
    MM(2, fc, xc); SB0; fc = wread(sb, 5); SB0;
    MM(3, fa, xc); SB0; fa = wread(sb, 6); SB0;
    MM(4, fb, xc); SB0; fb = wread(sb, 7); SB0;
    MM(5, fc, xc); SB0; fc = wread(sb, 8); SB0;
    if (!last) {
      if (s < KT1 - 2) WAIT_V(5); else WAIT_V(0);
      __builtin_amdgcn_s_barrier();
      asm volatile("" ::: "memory");
#pragma unroll
      for (int m = 0; m < 4; ++m) xn[m] = *(const bf16x8*)(sn + xoff + m * 1024);
      SB0;
    }
    MM(6, fa, xc); SB0; if (!last) fa = wread(sn, 0); SB0;
    if (!last) { if (s + 3 < KT1) issue(s + 3); }
    SB0;
    MM(7, fb, xc); SB0; if (!last) fb = wread(sn, 1); SB0;
    MM(8, fc, xc); SB0;
  };
  for (int s = 0; s < KT1 - 2; s += 2) {
    body(s, xa, xb, false);
    body(s + 1, xb, xa, false);
  }
  body(KT1 - 2, xa, xb, false);
  body(KT1 - 1, xb, xa, true);
#undef MM

  const int c = lane & 15;
  const int P0 = 9 * (ft >> 1) + 4 * (ft & 1) + 2 * wn;
  const int PX = 9 * (ft >> 1) + 8;
#pragma unroll
  for (int m = 0; m < 4; ++m) {
    const int tt = mt * 16 + wm * 4 + m;
    char* hb = (char*)h_t + (size_t)tt * KT2 * 1024;
#pragma unroll
    for (int n = 0; n < 2; ++n) {
      u32x2 a = swiglu4(acc[2 * n][m], acc[4 + 2 * n][m]);
      u32x2 b = swiglu4(acc[2 * n + 1][m], acc[5 + 2 * n][m]);
      st16u(hb + (size_t)(P0 + n) * 1024 + lane * 16, (u32x4){a[0], a[1], b[0], b[1]});
    }
    {
      f32x4 o;
#pragma unroll
      for (int r = 0; r < 4; ++r) o[r] = __shfl_xor(acc[8][m][r], 32, 64);
      if (lane < 32) {
        u32x2 w = swiglu4(acc[8][m], o);
        int gs = 2 * wn + g;
        *(u32x2*)(hb + (size_t)PX * 1024 + (gs * 16 + c) * 16 + (ft & 1) * 8) = w;
      }
    }
  }
  KEND(KID);
}

static constexpr int WST = 18432;
template <int KID>
__global__ __launch_bounds__(512, 2) void gemm2_kernel(const u16* __restrict__ h_t, const u16* __restrict__ w2,
                                                       float* __restrict__ part) {
  __shared__ __attribute__((aligned(16))) char lds[8 * WST];
  const int tid = threadIdx.x, lane = tid & 63;
  const int wave = __builtin_amdgcn_readfirstlane(tid >> 6);
  const int wm = wave & 3, wk = wave >> 2;
  const int bid = blockIdx.x, xcd = bid & 7, idx = bid >> 3;
  const int ks = xcd >> 1;
  const int nt = idx & 3, mt = (xcd & 1) * 8 + (idx >> 2);
  const int kt0 = ks * 36;
  KSTART(KID);

  const char* xs0 = (const char*)h_t + ((size_t)(mt * 8 + wm * 2) * KT2 + kt0 + wk * 18) * 1024 + lane * 16;
  const char* xs1 = xs0 + (size_t)KT2 * 1024;
  const char *ws0, *ws1, *ws2;
  {
    int ci = wave * 128 + lane;
    int img = ci / 576, rem = ci % 576, row = rem / 18, cc = rem % 18;
    ws0 = (const char*)(w2 + (size_t)((kt0 + img * 18) * 32 + row) * D_ + nt * 144 + cc * 8);
    ci += 64;
    img = ci / 576; rem = ci % 576; row = rem / 18; cc = rem % 18;
    ws1 = (const char*)(w2 + (size_t)((kt0 + img * 18) * 32 + row) * D_ + nt * 144 + cc * 8);
    ci = 1024 + wave * 16 + (lane & 15);
    img = ci / 576; rem = ci % 576; row = rem / 18; cc = rem % 18;
    ws2 = (const char*)(w2 + (size_t)((kt0 + img * 18) * 32 + row) * D_ + nt * 144 + cc * 8);
  }
  constexpr size_t WSTEP = (size_t)32 * D_ * 2;
  const unsigned lbase = lds_addr(lds);
  auto issueW = [&](int s) {
    unsigned sb = lbase + (s & 7) * WST;
    glds16(ws0, sb + wave * 2048);
    glds16(ws1, sb + wave * 2048 + 1024);
    if (lane < 16) glds16(ws2, sb + 16384 + wave * 256);
    ws0 += WSTEP; ws1 += WSTEP; ws2 += WSTEP;
    asm volatile("" : "+v"(ws0), "+v"(ws1), "+v"(ws2));
  };
  auto loadX = [&](bf16x8 (&x)[2]) {
    x[0] = gload16(xs0);
    x[1] = gload16(xs1);
    xs0 += 1024; xs1 += 1024;
    asm volatile("" : "+v"(xs0), "+v"(xs1));
  };

  f32x4 acc[9][2];
#pragma unroll
  for (int n = 0; n < 9; ++n) { acc[n][0] = (f32x4){0, 0, 0, 0}; acc[n][1] = (f32x4){0, 0, 0, 0}; }

  const int g = lane >> 4, q = (lane & 15) >> 2, p = lane & 3;
  const int woff = wk * 9216 + (4 * g + q) * 288 + (4 * p) * 2;

  bf16x8 xr[6][2];
#pragma unroll
  for (int j = 0; j < 5; ++j) { loadX(xr[j]); issueW(j); }
  issueW(5); issueW(6);
  wait_vm2<3 + 4 * 5 + 6>(xr[0][0], xr[0][1]);
  __builtin_amdgcn_s_barrier();
  asm volatile("" ::: "memory");

  auto wread = [&](const char* sb, int n) -> bf16x8 {
    const char* wp = sb + n * 32;
    return cat8(tr_read(wp), tr_read(wp + 16 * 288));
  };
#define SB0 __builtin_amdgcn_sched_barrier(0)
#define MM(n, w, x) do { acc[n][0] = __builtin_amdgcn_mfma_f32_16x16x32_bf16(w, x[0], acc[n][0], 0, 0, 0); \
                         acc[n][1] = __builtin_amdgcn_mfma_f32_16x16x32_bf16(w, x[1], acc[n][1], 0, 0, 0); } while (0)
  bf16x8 fa = wread(lds + woff, 0), fb = wread(lds + woff, 1), fc;
  auto stage = [&](int s, bf16x8 (&xc)[2], bf16x8 (&xn)[2], bf16x8 (&xp)[2], bool last) {
    const char* sb = lds + (s & 7) * WST + woff;
    const char* sn = lds + ((s + 1) & 7) * WST + woff;
    fc = wread(sb, 2); SB0;
    MM(0, fa, xc); fa = wread(sb, 3); SB0;
    MM(1, fb, xc); fb = wread(sb, 4); SB0;
    MM(2, fc, xc); fc = wread(sb, 5); SB0;
    MM(3, fa, xc); fa = wread(sb, 6); SB0;
    MM(4, fb, xc); fb = wread(sb, 7); SB0;
    MM(5, fc, xc); fc = wread(sb, 8); SB0;
    if (!last) {
      if (s <= 11) wait_vm<18>();
      else if (s == 12) wait_vm<15>();
      else if (s == 13) wait_vm<12>();
      else if (s == 14) wait_vm<7>();
      else if (s == 15) wait_vm<2>();
      else wait_vm<0>();
      asm volatile("" : "+v"(xn[0]), "+v"(xn[1]));
      __builtin_amdgcn_s_barrier();
      asm volatile("" ::: "memory");
      SB0;
    }
    MM(6, fa, xc); if (!last) fa = wread(sn, 0); SB0;
    if (s + 5 < 18) loadX(xp);
    if (s + 7 < 18) issueW(s + 7);
    SB0;
    MM(7, fb, xc); if (!last) fb = wread(sn, 1); SB0;
    MM(8, fc, xc); SB0;
  };
#pragma unroll 1
  for (int s = 0; s < 18; s += 6) {
    stage(s + 0, xr[0], xr[1], xr[5], false);
    stage(s + 1, xr[1], xr[2], xr[0], false);
    stage(s + 2, xr[2], xr[3], xr[1], false);
    stage(s + 3, xr[3], xr[4], xr[2], false);
    stage(s + 4, xr[4], xr[5], xr[3], false);
    stage(s + 5, xr[5], xr[0], xr[4], s + 5 == 17);
  }
#undef MM
  BAR();
  f32x4* red = (f32x4*)lds;
  int tz = threadIdx.x; asm volatile("" : "+v"(tz));
  const int ln = tz & 63;
#pragma unroll
  for (int n = 0; n < 9; ++n) {
    if ((n < 5) == (wk == 1)) {
#pragma unroll
      for (int m = 0; m < 2; ++m) red[(wm * 18 + n * 2 + m) * 64 + ln] = acc[n][m];
    }
  }
  BAR();
  {
    const int c = ln & 15;
#pragma unroll
    for (int m = 0; m < 2; ++m) {
      const int token = mt * 128 + wm * 32 + m * 16 + c;
      float* dst = part + ((size_t)ks * S_ + token) * D_ + nt * 144 + 4 * (ln >> 4);
#pragma unroll
      for (int n = 0; n < 9; ++n) {
        if ((n < 5) == (wk == 0)) {
          f32x4 o = acc[n][m] + red[(wm * 18 + n * 2 + m) * 64 + ln];
          st16(dst + n * 16, o);
        }
      }
    }
  }
  KEND(KID);
}

extern "C" void kernel_launch(void* const* d_in, const int* in_sizes, int n_in,
                              void* d_out, int out_size, void* d_ws, size_t ws_size,
                              hipStream_t stream) {
  const u16* A_q = (const u16*)d_in[0];
  const u16* A_k = (const u16*)d_in[1];
  const u16* A_v = (const u16*)d_in[2];
  const u16* A_shift = (const u16*)d_in[3];
  const u16* A_ssmk = (const u16*)d_in[4];
  const u16* A_ssmD = (const u16*)d_in[5];
  const u16* B_w1 = (const u16*)d_in[7];
  const u16* B_w3 = (const u16*)d_in[8];
  const u16* B_w2 = (const u16*)d_in[9];
  u16* out = (u16*)d_out;

  char* ws = (char*)d_ws;
  constexpr size_t XB = (size_t)S_ * D_ * 2;
  u16* y = (u16*)(ws);
  u16* xa = (u16*)(ws + XB);
  u16* xb = (u16*)(ws + 2 * XB);
  u16* xn = (u16*)(ws + 3 * XB);
  u16* h = (u16*)(ws + 4 * XB);
  float* part = (float*)(ws + 4 * XB + (size_t)S_ * F_ * 2);

  kvt_kernel<<<288, 256, 0, stream>>>(A_k, A_v, A_shift, xb);
  conv_kernel<<<D_ / 4, 512, 0, stream>>>(xb, A_ssmk, A_ssmD, y);
  row_kernel<0, 2><<<S_ / 4, 256, 0, stream>>>(A_q, nullptr, y, xa, xn);
  gemm1_kernel<3><<<256, 512, 0, stream>>>(xn, B_w1, B_w3, h);
  gemm2_kernel<4><<<256, 512, 0, stream>>>(h, B_w2, part);
  row_kernel<1, 5><<<S_ / 4, 256, 0, stream>>>(xa, part, y, xb, xn);
  gemm1_kernel<6><<<256, 512, 0, stream>>>(xn, B_w1, B_w3, h);
  gemm2_kernel<7><<<256, 512, 0, stream>>>(h, B_w2, part);
  row_kernel<2, 8><<<S_ / 4, 256, 0, stream>>>(xb, part, nullptr, out, nullptr);
}
```

```cpp
#include <hip/hip_runtime.h>
#include <hip/hip_bf16.h>
#include <stdint.h>
#define ST_SC1 1

typedef __attribute__((ext_vector_type(8))) short bf16x8;
typedef __attribute__((ext_vector_type(4))) short bf16x4;
typedef __attribute__((ext_vector_type(4))) float f32x4;
typedef __attribute__((ext_vector_type(4))) unsigned u32x4;
typedef __attribute__((ext_vector_type(2))) unsigned u32x2;
typedef unsigned short u16;

static constexpr int S_ = 2048, D_ = 576, F_ = 4608;
static constexpr int KT1 = D_ / 32;
static constexpr int KT2 = F_ / 32;
static constexpr int NSPLIT = 4;

#define LDS_AS __attribute__((address_space(3)))
#define GLB_AS __attribute__((address_space(1)))

__device__ __forceinline__ float bf2f(u16 v) { return __uint_as_float(((unsigned)v) << 16); }
__device__ __forceinline__ u16 f2bf(float f) {
  __hip_bfloat16 b = __float2bfloat16(f);
  return *reinterpret_cast<u16*>(&b);
}
__device__ __forceinline__ float rbf(float f) { return bf2f(f2bf(f)); }
__device__ __forceinline__ unsigned pack2(float lo, float hi) {
  return (unsigned)f2bf(lo) | ((unsigned)f2bf(hi) << 16);
}

typedef __attribute__((ext_vector_type(2))) float f32x2;
typedef __attribute__((ext_vector_type(2))) __bf16 xbf16x2;
__device__ __forceinline__ unsigned cvt2(float lo, float hi) {
  f32x2 v = {lo, hi};
  xbf16x2 b = __builtin_convertvector(v, xbf16x2);
  return __builtin_bit_cast(unsigned, b);
}
__device__ __forceinline__ float lo16(unsigned w) { return __uint_as_float(w << 16); }
__device__ __forceinline__ float hi16(unsigned w) { return __uint_as_float(w & 0xffff0000u); }
__device__ __forceinline__ unsigned lds_addr(const void* l) { return (unsigned)(size_t)(LDS_AS const char*)l; }
__device__ __forceinline__ void st8(void* p, u32x2 v) {
  asm volatile("global_store_dwordx2 %0, %1, off sc0 sc1" :: "v"(p), "v"(v) : "memory");
}
__device__ __forceinline__ void st16u(void* p, u32x4 v) {
  asm volatile("global_store_dwordx4 %0, %1, off sc0 sc1\n\ts_nop 1" :: "v"(p), "v"(v) : "memory");
}
__device__ __forceinline__ void st16(void* p, f32x4 v) {
  asm volatile("global_store_dwordx4 %0, %1, off sc0 sc1\n\ts_nop 1" :: "v"(p), "v"(v) : "memory");
}
__device__ __forceinline__ void glds16(const void* g, unsigned l) {
  unsigned keep;
  l = __builtin_amdgcn_readfirstlane(l);
  asm volatile("s_mov_b32 %0, m0\n\ts_mov_b32 m0, %2\n\ts_nop 0\n\tglobal_load_lds_dwordx4 %1, off\n\ts_mov_b32 m0, %0"
               : "=&s"(keep) : "v"(g), "s"(l) : "memory");
}
__device__ __forceinline__ bf16x4 tr_read(const char* p) {
  return __builtin_amdgcn_ds_read_tr16_b64_v4i16((LDS_AS bf16x4*)p);
}
__device__ __forceinline__ bf16x8 cat8(bf16x4 a, bf16x4 b) {
  return __builtin_shufflevector(a, b, 0, 1, 2, 3, 4, 5, 6, 7);
}
#define KSTART(id)
#define KEND(id)
#define WAIT_V(n) asm volatile("s_waitcnt vmcnt(" #n ")" ::: "memory")
#define BAR() do { asm volatile("s_waitcnt lgkmcnt(0)" ::: "memory"); __builtin_amdgcn_s_barrier(); asm volatile("" ::: "memory"); } while (0)

static constexpr int CV_NB = 48;
static constexpr int CV_KV_BYTES = 8 * CV_NB * 16;
static constexpr int CV_CS8 = 552;
static constexpr int CV_HC_BYTES = 4 * CV_CS8 * 8;

__global__ __launch_bounds__(256) void kvt_kernel(const u16* __restrict__ kin, const u16* __restrict__ vin,
                                                  const u16* __restrict__ shift_k, u16* __restrict__ kvt) {
  __shared__ __attribute__((aligned(16))) u16 tl[64][72];
  const int tid = threadIdx.x;
  KSTART(0);
  const int blk = blockIdx.x & 31, cht = blockIdx.x >> 5;
  const int cg = tid & 7, c0 = cht * 64 + cg * 8;
  float sk[8][4];
#pragma unroll
  for (int e = 0; e < 8; ++e) {
    u32x2 w = *(const u32x2*)(shift_k + (c0 + e) * 4);
    sk[e][0] = lo16(w[0]); sk[e][1] = hi16(w[0]); sk[e][2] = lo16(w[1]); sk[e][3] = hi16(w[1]);
  }
#pragma unroll
  for (int ps = 0; ps < 2; ++ps) {
    const int r = (tid >> 3) + 32 * ps, t = blk * 64 + r;
    u32x4 kr[4];
#pragma unroll
    for (int j = 0; j < 4; ++j) {
      int tt = t - 3 + j;
      if (tt >= 0) kr[j] = *(const u32x4*)(kin + (size_t)tt * D_ + c0);
      else kr[j] = (u32x4){0u, 0u, 0u, 0u};
    }
    u32x4 vr = *(const u32x4*)(vin + (size_t)t * D_ + c0);
    const int slot = (r & ~7) + 7 - (r & 7);
#pragma unroll
    for (int e = 0; e < 8; ++e) {
      float acc = 0.f;
#pragma unroll
      for (int j = 0; j < 4; ++j) {
        float kvv = (e & 1) ? hi16(kr[j][e >> 1]) : lo16(kr[j][e >> 1]);
        acc = rbf(acc + rbf(kvv * sk[e][3 - j]));
      }
      float vv = (e & 1) ? hi16(vr[e >> 1]) : lo16(vr[e >> 1]);
      tl[cg * 8 + e][slot] = f2bf(acc * vv);
    }
  }
  __syncthreads();
#pragma unroll
  for (int ps = 0; ps < 2; ++ps) {
    const int ch = (tid >> 3) + 32 * ps, pl = tid & 7;
    u32x4 w = *(const u32x4*)(&tl[ch][pl * 8]);
    *(u32x4*)(kvt + ((size_t)(cht * 64 + ch) * 32 + blk) * 64 + pl * 8) = w;
  }
  KEND(0);
}

struct ConvFr { bf16x4 G[8]; bf16x8 B[2]; };
__device__ __forceinline__ void conv_load(ConvFr& f, unsigned ha, unsigned ka) {
  asm volatile("ds_read_b64 %0, %10\n\tds_read_b64 %1, %10 offset:8\n\t"
               "ds_read_b64 %2, %10 offset:32\n\tds_read_b64 %3, %10 offset:40\n\t"
               "ds_read_b64 %4, %10 offset:64\n\tds_read_b64 %5, %10 offset:72\n\t"
               "ds_read_b64 %6, %10 offset:96\n\tds_read_b64 %7, %10 offset:104\n\t"
               "ds_read_b128 %8, %11\n\tds_read_b128 %9, %11 offset:3072"
               : "=&v"(f.G[0]), "=&v"(f.G[1]), "=&v"(f.G[2]), "=&v"(f.G[3]), "=&v"(f.G[4]), "=&v"(f.G[5]), "=&v"(f.G[6]), "=&v"(f.G[7]),
                 "=&v"(f.B[0]), "=&v"(f.B[1])
               : "v"(ha), "v"(ka) : "memory");
}
template <int N> __device__ __forceinline__ void conv_wait(ConvFr& f) {
  asm volatile("s_waitcnt lgkmcnt(%10)"
               : "+v"(f.G[0]), "+v"(f.G[1]), "+v"(f.G[2]), "+v"(f.G[3]), "+v"(f.G[4]), "+v"(f.G[5]), "+v"(f.G[6]), "+v"(f.G[7]),
                 "+v"(f.B[0]), "+v"(f.B[1]) : "i"(N) : "memory");
}
__device__ __forceinline__ void conv_mma(const ConvFr& f, bf16x8& Gm2, bf16x8& Gm1, f32x4 (&acc)[4]) {
  bf16x8 G0 = cat8(f.G[0], f.G[1]), G1 = cat8(f.G[2], f.G[3]), G2 = cat8(f.G[4], f.G[5]), G3 = cat8(f.G[6], f.G[7]);
  acc[0] = __builtin_amdgcn_mfma_f32_16x16x32_bf16(G0, f.B[0], acc[0], 0, 0, 0);
  acc[1] = __builtin_amdgcn_mfma_f32_16x16x32_bf16(G1, f.B[0], acc[1], 0, 0, 0);
  acc[2] = __builtin_amdgcn_mfma_f32_16x16x32_bf16(G2, f.B[0], acc[2], 0, 0, 0);
  acc[3] = __builtin_amdgcn_mfma_f32_16x16x32_bf16(G3, f.B[0], acc[3], 0, 0, 0);
  acc[0] = __builtin_amdgcn_mfma_f32_16x16x32_bf16(Gm2, f.B[1], acc[0], 0, 0, 0);
  acc[1] = __builtin_amdgcn_mfma_f32_16x16x32_bf16(Gm1, f.B[1], acc[1], 0, 0, 0);
  acc[2] = __builtin_amdgcn_mfma_f32_16x16x32_bf16(G0, f.B[1], acc[2], 0, 0, 0);
  acc[3] = __builtin_amdgcn_mfma_f32_16x16x32_bf16(G1, f.B[1], acc[3], 0, 0, 0);
  Gm2 = G2; Gm1 = G3;
}
__device__ __forceinline__ void conv_part(const char* hb, const char* kvb, int d0, int d1, f32x4 (&acc)[4]) {
  bf16x8 Gm2, Gm1;
  if (d0 == 0) { Gm2 = (bf16x8){0, 0, 0, 0, 0, 0, 0, 0}; Gm1 = Gm2; }
  else {
    const char* p = hb + (4 * d0 - 2) * 32;
    Gm2 = cat8(*(const bf16x4*)p, *(const bf16x4*)(p + 8));
    Gm1 = cat8(*(const bf16x4*)(p + 32), *(const bf16x4*)(p + 40));
  }
  unsigned ha = lds_addr(hb) + d0 * 128, ka = lds_addr(kvb) - d0 * 16;
  ConvFr fa, fb;
  conv_load(fa, ha, ka);
#pragma unroll 1
  for (int dl = d0; dl < d1; dl += 2) {
    conv_load(fb, ha + 128, ka - 16);
    conv_wait<10>(fa);
    conv_mma(fa, Gm2, Gm1, acc);
    __builtin_amdgcn_sched_barrier(0);
    ha += 256; ka -= 32;
    conv_load(fa, ha, ka);
    conv_wait<10>(fb);
    conv_mma(fb, Gm2, Gm1, acc);
    __builtin_amdgcn_sched_barrier(0);
  }
  conv_wait<0>(fa);
}

__global__ __launch_bounds__(512) void conv_kernel(const u16* __restrict__ kvt, const u16* __restrict__ ssm_k,
                                                   const u16* __restrict__ ssm_D, u16* __restrict__ yout,
                                                   const u16* __restrict__ w1, const u16* __restrict__ w3,
                                                   const u16* __restrict__ w2) {
  if (blockIdx.x >= D_ / 4) {
    const int nb = gridDim.x - D_ / 4, b = blockIdx.x - D_ / 4;
    constexpr int NCH = D_ * F_ / 8;
    u32x4 a = (u32x4){0u, 0u, 0u, 0u};
    for (int i = b * 512 + threadIdx.x; i < NCH; i += nb * 512) {
      a ^= *(const u32x4*)(w1 + (size_t)i * 8);
      a ^= *(const u32x4*)(w3 + (size_t)i * 8);
      a ^= *(const u32x4*)(w2 + (size_t)i * 8);
    }
    if (a[0] == 0x9e3779b9u && a[1] == 0x7f4a7c15u && a[2] == 0x85ebca6bu && a[3] == 0xc2b2ae35u) yout[0] = 0;
    return;
  }
  __shared__ __attribute__((aligned(16))) char lds[4 * CV_KV_BYTES + 4 * CV_HC_BYTES + 16384];
  char* kvs = lds;
  char* hcs = lds + 4 * CV_KV_BYTES;
  char* red = lds + 4 * CV_KV_BYTES + 4 * CV_HC_BYTES;
  const int tid = threadIdx.x, lane = tid & 63, wave = tid >> 6;
  const int d0 = blockIdx.x * 4;
  KSTART(1);
  const int t0 = tid * 4;
#pragma unroll
  for (int ps = 0; ps < 2; ++ps) {
    int it = tid + 512 * ps, c = it >> 8, blk = (it >> 3) & 31, pl = it & 7;
    u32x4 w = *(const u32x4*)(kvt + ((size_t)(d0 + c) * 32 + blk) * 64 + pl * 8);
    *(u32x4*)(kvs + c * CV_KV_BYTES + (pl * CV_NB + blk + 16) * 16) = w;
  }
  for (int it = tid; it < 4 * 264; it += 512) {
    int c = it / 264, qp = it % 264;
    int q = qp - 4;
    const u16* hp = ssm_k + (size_t)(d0 + c) * S_;
    u32x4 a = (u32x4){0u, 0u, 0u, 0u}, b = (u32x4){0u, 0u, 0u, 0u};
    if (q >= 0 && q < 256) a = *(const u32x4*)(hp + q * 8);
    if (q + 1 >= 0 && q + 1 < 256) b = *(const u32x4*)(hp + (q + 1) * 8);
    char* dst = hcs + c * CV_HC_BYTES + qp * 16;
    *(u32x4*)(dst + 0 * CV_CS8 * 8) = a;
    u32x4 s1;
    s1[0] = __builtin_amdgcn_alignbit(a[1], a[0], 16);
    s1[1] = __builtin_amdgcn_alignbit(a[2], a[1], 16);
    s1[2] = __builtin_amdgcn_alignbit(a[3], a[2], 16);
    s1[3] = __builtin_amdgcn_alignbit(b[0], a[3], 16);
    *(u32x4*)(dst + 1 * CV_CS8 * 8) = s1;
    u32x4 s2 = (u32x4){a[1], a[2], a[3], b[0]};
    *(u32x4*)(dst + 2 * CV_CS8 * 8) = s2;
    u32x4 s3;
    s3[0] = s1[1]; s3[1] = s1[2]; s3[2] = s1[3];
    s3[3] = __builtin_amdgcn_alignbit(b[1], b[0], 16);
    *(u32x4*)(dst + 3 * CV_CS8 * 8) = s3;
  }
  {
    int c = tid >> 7, pl = (tid >> 4) & 7, b = tid & 15;
    *(u32x4*)(kvs + c * CV_KV_BYTES + (pl * CV_NB + b) * 16) = (u32x4){0u, 0u, 0u, 0u};
  }
  float dd[4];
#pragma unroll
  for (int c = 0; c < 4; ++c) dd[c] = bf2f(ssm_D[d0 + c]);
  __syncthreads();

  const int ch = wave & 3, part = wave >> 2;
  const int c16 = lane & 15, g = lane >> 4;
  f32x4 accA[4], accB[4];
#pragma unroll
  for (int i = 0; i < 4; ++i) { accA[i] = (f32x4){0, 0, 0, 0}; accB[i] = (f32x4){0, 0, 0, 0}; }
  {
    const char* kvb = kvs + ch * CV_KV_BYTES + (g * CV_NB + 16 + c16) * 16;
    const int r = (c16 + 1) & 3, w = (c16 + 1) >> 2;
    const char* hb = hcs + ch * CV_HC_BYTES + (r * CV_CS8 - 2 * g + 6 + w) * 8;
    if (part == 0) {
      conv_part(hb, kvb + 16 * 16, 0, 24, accA);
    } else {
      conv_part(hb, kvb, 0, 16, accA);
      conv_part(hb, kvb + 16 * 16, 24, 32, accB);
#pragma unroll
      for (int i = 0; i < 4; ++i) *(f32x4*)(red + ((ch * 4 + i) * 64 + lane) * 16) = accB[i];
    }
  }
  __syncthreads();
  {
    char* yt = hcs + ch * 4096;
    const int ct = part ? 0 : 1;
#pragma unroll
    for (int i = 0; i < 4; ++i) {
      f32x4 a = accA[i];
      if (part == 0) a += *(const f32x4*)(red + ((ch * 4 + i) * 64 + lane) * 16);
      int t = 64 * (16 * ct + c16) + 16 * i + 4 * g;
      u32x2 w;
      w[0] = pack2(a[0], a[1]);
      w[1] = pack2(a[2], a[3]);
      *(u32x2*)(yt + t * 2) = w;
    }
  }
  __syncthreads();
  {
    u32x2 yc[4];
    float kvf[4][4];
    const int plane = (t0 & 63) >> 3, blk = (t0 >> 6) + 16, hi = (t0 >> 2) & 1;
#pragma unroll
    for (int c = 0; c < 4; ++c) {
      yc[c] = *(const u32x2*)(hcs + c * 4096 + t0 * 2);
      u32x2 kw = *(const u32x2*)(kvs + c * CV_KV_BYTES + (plane * CV_NB + blk) * 16 + (hi ? 0 : 8));
      kvf[c][3] = lo16(kw[0]); kvf[c][2] = hi16(kw[0]); kvf[c][1] = lo16(kw[1]); kvf[c][0] = hi16(kw[1]);
    }
#pragma unroll
    for (int i = 0; i < 4; ++i) {
      float o[4];
#pragma unroll
      for (int c = 0; c < 4; ++c) {
        unsigned w = yc[c][i >> 1];
        float yv = bf2f((u16)((i & 1) ? (w >> 16) : (w & 0xffff)));
        o[c] = yv + rbf(kvf[c][i] * dd[c]);
      }
      u32x2 w;
      w[0] = pack2(o[0], o[1]);
      w[1] = pack2(o[2], o[3]);
      *(u32x2*)(yout + (size_t)(t0 + i) * D_ + d0) = w;
    }
  }
  KEND(1);
}

__device__ __forceinline__ float sum64(float v) {
#pragma unroll
  for (int o = 1; o < 64; o <<= 1) v += __shfl_xor(v, o, 64);
  return v;
}
__device__ __forceinline__ void layernorm9(const float (&x)[9], float (&o)[9]) {
  float s = 0.f;
#pragma unroll
  for (int i = 0; i < 9; ++i) s += x[i];
  s = sum64(s);
  const float mean = s * (1.0f / D_);
  float q = 0.f;
#pragma unroll
  for (int i = 0; i < 9; ++i) { float d = x[i] - mean; q += d * d; }
  q = sum64(q);
  const float var = q * (1.0f / D_);
  const float mb = rbf(mean);
  const float vb = rbf(var);
  const float sd = rbf(sqrtf(rbf(vb + 1e-5f)));
#pragma unroll
  for (int i = 0; i < 9; ++i) o[i] = rbf(rbf(x[i] - mb) / sd);
}
__device__ __forceinline__ void load9(const u16* __restrict__ p, int l, float (&x)[9]) {
  u32x2 a = *(const u32x2*)(p + 4 * l), b = *(const u32x2*)(p + 256 + 4 * l);
  u16 c = p[512 + l];
  x[0] = lo16(a[0]); x[1] = hi16(a[0]); x[2] = lo16(a[1]); x[3] = hi16(a[1]);
  x[4] = lo16(b[0]); x[5] = hi16(b[0]); x[6] = lo16(b[1]); x[7] = hi16(b[1]);
  x[8] = bf2f(c);
}

template <int MODE, int KID>
__global__ __launch_bounds__(256) void row_kernel(const u16* __restrict__ xin, const float* __restrict__ part,
                                                  const u16* __restrict__ y, u16* __restrict__ xout,
                                                  u16* __restrict__ xn_t) {
  KSTART(KID);
  const int l = threadIdx.x & 63;
  const int row = blockIdx.x * 4 + (threadIdx.x >> 6);
  float x[9], yv[9];
  load9(xin + (size_t)row * D_, l, x);
  if (MODE <= 1) load9(y + (size_t)row * D_, l, yv);
  if (MODE >= 1) {
    f32x4 a = (f32x4){0, 0, 0, 0}, b = (f32x4){0, 0, 0, 0};
    float c = 0.f;
#pragma unroll
    for (int s = 0; s < NSPLIT; ++s) {
      const float* pp = part + ((size_t)s * S_ + row) * D_;
      a += *(const f32x4*)(pp + 4 * l);
      b += *(const f32x4*)(pp + 256 + 4 * l);
      c += pp[512 + l];
    }
#pragma unroll
    for (int e = 0; e < 4; ++e) { x[e] = rbf(x[e] + rbf(a[e])); x[4 + e] = rbf(x[4 + e] + rbf(b[e])); }
    x[8] = rbf(x[8] + rbf(c));
  }
  if (MODE <= 1) {
    float n[9];
    layernorm9(x, n);
#pragma unroll
    for (int i = 0; i < 9; ++i) x[i] = rbf(x[i] + rbf(n[i] * yv[i]));
  }
  {
    u16* po = xout + (size_t)row * D_;
    u32x2 w;
    w[0] = cvt2(x[0], x[1]); w[1] = cvt2(x[2], x[3]);
    *(u32x2*)(po + 4 * l) = w;
    w[0] = cvt2(x[4], x[5]); w[1] = cvt2(x[6], x[7]);
    *(u32x2*)(po + 256 + 4 * l) = w;
    po[512 + l] = f2bf(x[8]);
  }
  if (MODE <= 1) {
    float n[9];
    layernorm9(x, n);
    const int tt = row >> 4, c = row & 15;
    char* base = (char*)xn_t + (size_t)tt * KT1 * 1024 + c * 16;
    u32x2 w;
    w[0] = cvt2(n[0], n[1]); w[1] = cvt2(n[2], n[3]);
    *(u32x2*)(base + (l >> 3) * 1024 + (l & 3) * 256 + ((l >> 2) & 1) * 8) = w;
    w[0] = cvt2(n[4], n[5]); w[1] = cvt2(n[6], n[7]);
    *(u32x2*)(base + (8 + (l >> 3)) * 1024 + (l & 3) * 256 + ((l >> 2) & 1) * 8) = w;
    *(u16*)(base + (16 + (l >> 5)) * 1024 + ((l >> 2) & 3) * 256 + ((l >> 4) & 1) * 8 + (l & 3) * 2) = f2bf(n[8]);
  }
  KEND(KID);
}

static constexpr int STG = 34816;
static constexpr int NSTG = 4;

__device__ __forceinline__ u32x2 silu_mul4(f32x4 g, f32x4 u) {
  u32x2 o;
#pragma unroll
  for (int i = 0; i < 2; ++i) {
    unsigned gw = cvt2(g[2 * i], g[2 * i + 1]);
    unsigned uw = cvt2(u[2 * i], u[2 * i + 1]);
    float g0 = lo16(gw), g1 = hi16(gw);
    float s0 = g0 * __builtin_amdgcn_rcpf(1.0f + __builtin_amdgcn_exp2f(g0 * -1.44269504089f));
    float s1 = g1 * __builtin_amdgcn_rcpf(1.0f + __builtin_amdgcn_exp2f(g1 * -1.44269504089f));
    unsigned sw = cvt2(s0, s1);
    o[i] = cvt2(lo16(sw) * lo16(uw), hi16(sw) * hi16(uw));
  }
  return o;
}

template <int N> __device__ __forceinline__ void wait_vm2(bf16x8& a, bf16x8& b) {
  asm volatile("s_waitcnt vmcnt(%2)" : "+v"(a), "+v"(b) : "i"(N) : "memory");
}
__device__ __forceinline__ void wait_vm2_dyn(int n, bf16x8& a, bf16x8& b) {
  switch (n) {
    case 0: wait_vm2<0>(a, b); break;
    case 1: wait_vm2<1>(a, b); break;
    case 2: wait_vm2<2>(a, b); break;
    case 3: wait_vm2<3>(a, b); break;
    case 4: wait_vm2<4>(a, b); break;
    case 5: wait_vm2<5>(a, b); break;
    case 6: wait_vm2<6>(a, b); break;
    case 7: wait_vm2<7>(a, b); break;
    case 8: wait_vm2<8>(a, b); break;
    case 9: wait_vm2<9>(a, b); break;
    case 10: wait_vm2<10>(a, b); break;
    case 11: wait_vm2<11>(a, b); break;
    case 12: wait_vm2<12>(a, b); break;
    case 13: wait_vm2<13>(a, b); break;
    case 14: wait_vm2<14>(a, b); break;
    case 15: wait_vm2<15>(a, b); break;
    case 16: wait_vm2<16>(a, b); break;
    case 17: wait_vm2<17>(a, b); break;
    case 18: wait_vm2<18>(a, b); break;
    case 19: wait_vm2<19>(a, b); break;
    case 20: wait_vm2<20>(a, b); break;
    case 21: wait_vm2<21>(a, b); break;
    case 22: wait_vm2<22>(a, b); break;
    case 23: wait_vm2<23>(a, b); break;
    case 24: wait_vm2<24>(a, b); break;
    case 25: wait_vm2<25>(a, b); break;
    case 26: wait_vm2<26>(a, b); break;
    case 27: wait_vm2<27>(a, b); break;
    case 28: wait_vm2<28>(a, b); break;
    case 29: wait_vm2<29>(a, b); break;
    case 30: wait_vm2<30>(a, b); break;
    case 31: wait_vm2<31>(a, b); break;
    default: wait_vm2<0>(a, b); break;
  }
}
template <int N> __device__ __forceinline__ void wait_vm() {
  asm volatile("s_waitcnt vmcnt(%0)" :: "i"(N) : "memory");
}
__device__ __forceinline__ bf16x8 gload16(const void* p) {
  bf16x8 r;
  asm volatile("global_load_dwordx4 %0, %1, off" : "=v"(r) : "v"(p) : "memory");
  return r;
}
__device__ __forceinline__ u32x2 swiglu4(f32x4 g, f32x4 u) {
  f32x4 e;
#pragma unroll
  for (int i = 0; i < 4; ++i) e[i] = __builtin_amdgcn_exp2f(g[i] * -1.44269504089f);
  e = e + 1.0f;
#pragma unroll
  for (int i = 0; i < 4; ++i) e[i] = __builtin_amdgcn_rcpf(e[i]);
  f32x4 h = (g * u) * e;
  u32x2 o;
  o[0] = cvt2(h[0], h[1]);
  o[1] = cvt2(h[2], h[3]);
  return o;
}

template <int KID>
__global__ __launch_bounds__(512, 2) void gemm1_kernel(const u16* __restrict__ xn_t, const u16* __restrict__ w1,
                                                       const u16* __restrict__ w3, u16* __restrict__ h_t) {
  __shared__ __attribute__((aligned(16))) char lds[NSTG * STG];
  const int tid = threadIdx.x, lane = tid & 63;
  const int wave = __builtin_amdgcn_readfirstlane(tid >> 6);
  const int wm = wave & 3, wn = wave >> 2;
  const int bid = blockIdx.x, xcd = bid & 7, idx = bid >> 3;
  const int ft = xcd * 4 + (idx & 3), mt = idx >> 2;
  KSTART(KID);
  const char* xs = (const char*)xn_t + ((size_t)(mt * 16 + 2 * wave) * KT1) * 1024 + lane * 16;
  const char *ws0, *ws1, *ws2;
  {
    const int colA = 32 * (9 * (ft >> 1) + 4 * (ft & 1)), colB = 32 * (9 * (ft >> 1) + 8) + 16 * (ft & 1) - 128;
    int ci = wave * 128 + lane;
    int img = ci / 576, rem = ci % 576, row = rem / 18, cc = rem % 18;
    ws0 = (const char*)((img ? w3 : w1) + (size_t)row * F_ + (cc < 16 ? colA : colB) + cc * 8);
    ci += 64;
    img = ci / 576; rem = ci % 576; row = rem / 18; cc = rem % 18;
    ws1 = (const char*)((img ? w3 : w1) + (size_t)row * F_ + (cc < 16 ? colA : colB) + cc * 8);
    ci = 1024 + wave * 16 + (lane & 15);
    img = ci / 576; rem = ci % 576; row = rem / 18; cc = rem % 18;
    ws2 = (const char*)((img ? w3 : w1) + (size_t)row * F_ + (cc < 16 ? colA : colB) + cc * 8);
  }
  constexpr size_t WSTEP = (size_t)32 * F_ * 2;
  const unsigned lbase = lds_addr(lds);
  auto issue = [&](int s) {
    unsigned sb = lbase + (s & 3) * STG;
    glds16(xs + s * 1024, sb + (2 * wave) * 1024);
    glds16(xs + KT1 * 1024 + s * 1024, sb + (2 * wave + 1) * 1024);
    glds16(ws0 + s * WSTEP, sb + 16384 + wave * 2048);
    glds16(ws1 + s * WSTEP, sb + 16384 + wave * 2048 + 1024);
    if (lane < 16) glds16(ws2 + s * WSTEP, sb + 16384 + 16384 + wave * 256);
  };

  f32x4 acc[9][4];
#pragma unroll
  for (int n = 0; n < 9; ++n)
#pragma unroll
    for (int m = 0; m < 4; ++m) acc[n][m] = (f32x4){0, 0, 0, 0};

  const int g = lane >> 4, q = (lane & 15) >> 2, p = lane & 3;
  const int cb = wn * 64, cm = 128 + 8 * wn;
  const int xoff = wm * 4096 + lane * 16;
  const int woff = 16384 + (4 * g + q) * 288 + (cb + 4 * p) * 2;
  const int moff = 16384 + (p >> 1) * 9216 + (4 * g + q) * 288 + (cm + 4 * (p & 1)) * 2;

  issue(0); issue(1); issue(2);
  auto wread = [&](const char* sb, int n) -> bf16x8 {
    const char* wp = (n < 8) ? (sb + woff + (n >> 2) * 9216 + (n & 3) * 32) : (sb + moff);
    return cat8(tr_read(wp), tr_read(wp + 16 * 288));
  };
  bf16x8 xa[4], xb[4], fa, fb, fc;
  WAIT_V(10);
  __builtin_amdgcn_s_barrier();
  asm volatile("" ::: "memory");
#pragma unroll
  for (int m = 0; m < 4; ++m) xa[m] = *(const bf16x8*)(lds + xoff + m * 1024);
  fa = wread(lds, 0);
  fb = wread(lds, 1);

#define SB0 __builtin_amdgcn_sched_barrier(0)
#define MM(n, w, x) _Pragma("unroll") for (int m = 0; m < 4; ++m) acc[n][m] = __builtin_amdgcn_mfma_f32_16x16x32_bf16(w, x[m], acc[n][m], 0, 0, 0)
  auto body = [&](int s, bf16x8 (&xc)[4], bf16x8 (&xn)[4], bool last) {
    const char* sb = lds + (s & 3) * STG;
    const char* sn = lds + ((s + 1) & 3) * STG;
    fc = wread(sb, 2); SB0;
    MM(0, fa, xc); SB0; fa = wread(sb, 3); SB0;
    MM(1, fb, xc); SB0; fb = wread(sb, 4); SB0;
    MM(2, fc, xc); SB0; fc = wread(sb, 5); SB0;
    MM(3, fa, xc); SB0; fa = wread(sb, 6); SB0;
    MM(4, fb, xc); SB0; fb = wread(sb, 7); SB0;
    MM(5, fc, xc); SB0; fc = wread(sb, 8); SB0;
    if (!last) {
      if (s < KT1 - 2) WAIT_V(5); else WAIT_V(0);
      __builtin_amdgcn_s_barrier();
      asm volatile("" ::: "memory");
#pragma unroll
      for (int m = 0; m < 4; ++m) xn[m] = *(const bf16x8*)(sn + xoff + m * 1024);
      SB0;
    }
    MM(6, fa, xc); SB0; if (!last) fa = wread(sn, 0); SB0;
    if (!last) { if (s + 3 < KT1) issue(s + 3); }
    SB0;
    MM(7, fb, xc); SB0; if (!last) fb = wread(sn, 1); SB0;
    MM(8, fc, xc); SB0;
  };
  for (int s = 0; s < KT1 - 2; s += 2) {
    body(s, xa, xb, false);
    body(s + 1, xb, xa, false);
  }
  body(KT1 - 2, xa, xb, false);
  body(KT1 - 1, xb, xa, true);
#undef MM

  const int c = lane & 15;
  const int P0 = 9 * (ft >> 1) + 4 * (ft & 1) + 2 * wn;
  const int PX = 9 * (ft >> 1) + 8;
#pragma unroll
  for (int m = 0; m < 4; ++m) {
    const int tt = mt * 16 + wm * 4 + m;
    char* hb = (char*)h_t + (size_t)tt * KT2 * 1024;
#pragma unroll
    for (int n = 0; n < 2; ++n) {
      u32x2 a = swiglu4(acc[2 * n][m], acc[4 + 2 * n][m]);
      u32x2 b = swiglu4(acc[2 * n + 1][m], acc[5 + 2 * n][m]);
      st16u(hb + (size_t)(P0 + n) * 1024 + lane * 16, (u32x4){a[0], a[1], b[0], b[1]});
    }
    {
      f32x4 o;
#pragma unroll
      for (int r = 0; r < 4; ++r) o[r] = __shfl_xor(acc[8][m][r], 32, 64);
      if (lane < 32) {
        u32x2 w = swiglu4(acc[8][m], o);
        int gs = 2 * wn + g;
        *(u32x2*)(hb + (size_t)PX * 1024 + (gs * 16 + c) * 16 + (ft & 1) * 8) = w;
      }
    }
  }
  KEND(KID);
}

static constexpr int WST = 18432;
template <int KID>
__global__ __launch_bounds__(512, 2) void gemm2_kernel(const u16* __restrict__ h_t, const u16* __restrict__ w2,
                                                       float* __restrict__ part) {
  __shared__ __attribute__((aligned(16))) char lds[8 * WST];
  const int tid = threadIdx.x, lane = tid & 63;
  const int wave = __builtin_amdgcn_readfirstlane(tid >> 6);
  const int wm = wave & 3, wk = wave >> 2;
  const int bid = blockIdx.x, xcd = bid & 7, idx = bid >> 3;
  const int ks = xcd >> 1;
  const int nt = idx & 3, mt = (xcd & 1) * 8 + (idx >> 2);
  const int kt0 = ks * 36;
  KSTART(KID);

  const char* xs0 = (const char*)h_t + ((size_t)(mt * 8 + wm * 2) * KT2 + kt0 + wk * 18) * 1024 + lane * 16;
  const char* xs1 = xs0 + (size_t)KT2 * 1024;
  const char *ws0, *ws1, *ws2;
  {
    int ci = wave * 128 + lane;
    int img = ci / 576, rem = ci % 576, row = rem / 18, cc = rem % 18;
    ws0 = (const char*)(w2 + (size_t)((kt0 + img * 18) * 32 + row) * D_ + nt * 144 + cc * 8);
    ci += 64;
    img = ci / 576; rem = ci % 576; row = rem / 18; cc = rem % 18;
    ws1 = (const char*)(w2 + (size_t)((kt0 + img * 18) * 32 + row) * D_ + nt * 144 + cc * 8);
    ci = 1024 + wave * 16 + (lane & 15);
    img = ci / 576; rem = ci % 576; row = rem / 18; cc = rem % 18;
    ws2 = (const char*)(w2 + (size_t)((kt0 + img * 18) * 32 + row) * D_ + nt * 144 + cc * 8);
  }
  constexpr size_t WSTEP = (size_t)32 * D_ * 2;
  const unsigned lbase = lds_addr(lds);
  auto issueW = [&](int s) {
    unsigned sb = lbase + (s & 7) * WST;
    glds16(ws0, sb + wave * 2048);
    glds16(ws1, sb + wave * 2048 + 1024);
    if (lane < 16) glds16(ws2, sb + 16384 + wave * 256);
    ws0 += WSTEP; ws1 += WSTEP; ws2 += WSTEP;
    asm volatile("" : "+v"(ws0), "+v"(ws1), "+v"(ws2));
  };
  auto loadX = [&](bf16x8 (&x)[2]) {
    x[0] = gload16(xs0);
    x[1] = gload16(xs1);
    xs0 += 1024; xs1 += 1024;
    asm volatile("" : "+v"(xs0), "+v"(xs1));
  };

  f32x4 acc[9][2];
#pragma unroll
  for (int n = 0; n < 9; ++n) { acc[n][0] = (f32x4){0, 0, 0, 0}; acc[n][1] = (f32x4){0, 0, 0, 0}; }

  const int g = lane >> 4, q = (lane & 15) >> 2, p = lane & 3;
  const int woff = wk * 9216 + (4 * g + q) * 288 + (4 * p) * 2;

  bf16x8 xr[6][2];
#pragma unroll
  for (int j = 0; j < 5; ++j) { loadX(xr[j]); issueW(j); }
  issueW(5); issueW(6);
  wait_vm2<3 + 4 * 5 + 6>(xr[0][0], xr[0][1]);
  __builtin_amdgcn_s_barrier();
  asm volatile("" ::: "memory");

  auto wread = [&](const char* sb, int n) -> bf16x8 {
    const char* wp = sb + n * 32;
    return cat8(tr_read(wp), tr_read(wp + 16 * 288));
  };
#define SB0 __builtin_amdgcn_sched_barrier(0)
#define MM(n, w, x) do { acc[n][0] = __builtin_amdgcn_mfma_f32_16x16x32_bf16(w, x[0], acc[n][0], 0, 0, 0); \
                         acc[n][1] = __builtin_amdgcn_mfma_f32_16x16x32_bf16(w, x[1], acc[n][1], 0, 0, 0); } while (0)
  bf16x8 fa = wread(lds + woff, 0), fb = wread(lds + woff, 1), fc;
  auto stage = [&](int s, bf16x8 (&xc)[2], bf16x8 (&xn)[2], bf16x8 (&xp)[2], bool last) {
    const char* sb = lds + (s & 7) * WST + woff;
    const char* sn = lds + ((s + 1) & 7) * WST + woff;
    fc = wread(sb, 2); SB0;
    MM(0, fa, xc); fa = wread(sb, 3); SB0;
    MM(1, fb, xc); fb = wread(sb, 4); SB0;
    MM(2, fc, xc); fc = wread(sb, 5); SB0;
    MM(3, fa, xc); fa = wread(sb, 6); SB0;
    MM(4, fb, xc); fb = wread(sb, 7); SB0;
    MM(5, fc, xc); fc = wread(sb, 8); SB0;
    if (!last) {
      if (s <= 11) wait_vm<18>();
      else if (s == 12) wait_vm<15>();
      else if (s == 13) wait_vm<12>();
      else if (s == 14) wait_vm<7>();
      else if (s == 15) wait_vm<2>();
      else wait_vm<0>();
      asm volatile("" : "+v"(xn[0]), "+v"(xn[1]));
      __builtin_amdgcn_s_barrier();
      asm volatile("" ::: "memory");
      SB0;
    }
    MM(6, fa, xc); if (!last) fa = wread(sn, 0); SB0;
    if (s + 5 < 18) loadX(xp);
    if (s + 7 < 18) issueW(s + 7);
    SB0;
    MM(7, fb, xc); if (!last) fb = wread(sn, 1); SB0;
    MM(8, fc, xc); SB0;
  };
#pragma unroll 1
  for (int s = 0; s < 18; s += 6) {
    stage(s + 0, xr[0], xr[1], xr[5], false);
    stage(s + 1, xr[1], xr[2], xr[0], false);
    stage(s + 2, xr[2], xr[3], xr[1], false);
    stage(s + 3, xr[3], xr[4], xr[2], false);
    stage(s + 4, xr[4], xr[5], xr[3], false);
    stage(s + 5, xr[5], xr[0], xr[4], s + 5 == 17);
  }
#undef MM
  BAR();
  f32x4* red = (f32x4*)lds;
  int tz = threadIdx.x; asm volatile("" : "+v"(tz));
  const int ln = tz & 63;
#pragma unroll
  for (int n = 0; n < 9; ++n) {
    if ((n < 5) == (wk == 1)) {
#pragma unroll
      for (int m = 0; m < 2; ++m) red[(wm * 18 + n * 2 + m) * 64 + ln] = acc[n][m];
    }
  }
  BAR();
  {
    const int c = ln & 15;
#pragma unroll
    for (int m = 0; m < 2; ++m) {
      const int token = mt * 128 + wm * 32 + m * 16 + c;
      float* dst = part + ((size_t)ks * S_ + token) * D_ + nt * 144 + 4 * (ln >> 4);
#pragma unroll
      for (int n = 0; n < 9; ++n) {
        if ((n < 5) == (wk == 0)) {
          f32x4 o = acc[n][m] + red[(wm * 18 + n * 2 + m) * 64 + ln];
          st16(dst + n * 16, o);
        }
      }
    }
  }
  KEND(KID);
}

extern "C" void kernel_launch(void* const* d_in, const int* in_sizes, int n_in,
                              void* d_out, int out_size, void* d_ws, size_t ws_size,
                              hipStream_t stream) {
  const u16* A_q = (const u16*)d_in[0];
  const u16* A_k = (const u16*)d_in[1];
  const u16* A_v = (const u16*)d_in[2];
  const u16* A_shift = (const u16*)d_in[3];
  const u16* A_ssmk = (const u16*)d_in[4];
  const u16* A_ssmD = (const u16*)d_in[5];
  const u16* B_w1 = (const u16*)d_in[7];
  const u16* B_w3 = (const u16*)d_in[8];
  const u16* B_w2 = (const u16*)d_in[9];
  u16* out = (u16*)d_out;

  char* ws = (char*)d_ws;
  constexpr size_t XB = (size_t)S_ * D_ * 2;
  u16* y = (u16*)(ws);
  u16* xa = (u16*)(ws + XB);
  u16* xb = (u16*)(ws + 2 * XB);
  u16* xn = (u16*)(ws + 3 * XB);
  u16* h = (u16*)(ws + 4 * XB);
  float* part = (float*)(ws + 4 * XB + (size_t)S_ * F_ * 2);

  kvt_kernel<<<288, 256, 0, stream>>>(A_k, A_v, A_shift, xb);
  conv_kernel<<<256, 512, 0, stream>>>(xb, A_ssmk, A_ssmD, y, B_w1, B_w3, B_w2);
  row_kernel<0, 2><<<S_ / 4, 256, 0, stream>>>(A_q, nullptr, y, xa, xn);
  gemm1_kernel<3><<<256, 512, 0, stream>>>(xn, B_w1, B_w3, h);
  gemm2_kernel<4><<<256, 512, 0, stream>>>(h, B_w2, part);
  row_kernel<1, 5><<<S_ / 4, 256, 0, stream>>>(xa, part, y, xb, xn);
  gemm1_kernel<6><<<256, 512, 0, stream>>>(xn, B_w1, B_w3, h);
  gemm2_kernel<7><<<256, 512, 0, stream>>>(h, B_w2, part);
  row_kernel<2, 8><<<S_ / 4, 256, 0, stream>>>(xb, part, nullptr, out, nullptr);
}
```

```cpp
#include <hip/hip_runtime.h>
#include <hip/hip_bf16.h>
#include <stdint.h>
#define ST_SC1 1

typedef __attribute__((ext_vector_type(8))) short bf16x8;
typedef __attribute__((ext_vector_type(4))) short bf16x4;
typedef __attribute__((ext_vector_type(4))) float f32x4;
typedef __attribute__((ext_vector_type(4))) unsigned u32x4;
typedef __attribute__((ext_vector_type(2))) unsigned u32x2;
typedef unsigned short u16;

static constexpr int S_ = 2048, D_ = 576, F_ = 4608;
static constexpr int KT1 = D_ / 32;
static constexpr int KT2 = F_ / 32;
static constexpr int NSPLIT = 4;

#define LDS_AS __attribute__((address_space(3)))
#define GLB_AS __attribute__((address_space(1)))

__device__ __forceinline__ float bf2f(u16 v) { return __uint_as_float(((unsigned)v) << 16); }
__device__ __forceinline__ u16 f2bf(float f) {
  __hip_bfloat16 b = __float2bfloat16(f);
  return *reinterpret_cast<u16*>(&b);
}
__device__ __forceinline__ float rbf(float f) { return bf2f(f2bf(f)); }
__device__ __forceinline__ unsigned pack2(float lo, float hi) {
  return (unsigned)f2bf(lo) | ((unsigned)f2bf(hi) << 16);
}

typedef __attribute__((ext_vector_type(2))) float f32x2;
typedef __attribute__((ext_vector_type(2))) __bf16 xbf16x2;
__device__ __forceinline__ unsigned cvt2(float lo, float hi) {
  f32x2 v = {lo, hi};
  xbf16x2 b = __builtin_convertvector(v, xbf16x2);
  return __builtin_bit_cast(unsigned, b);
}
__device__ __forceinline__ float lo16(unsigned w) { return __uint_as_float(w << 16); }
__device__ __forceinline__ float hi16(unsigned w) { return __uint_as_float(w & 0xffff0000u); }
__device__ __forceinline__ unsigned lds_addr(const void* l) { return (unsigned)(size_t)(LDS_AS const char*)l; }
__device__ __forceinline__ void st8(void* p, u32x2 v) {
  asm volatile("global_store_dwordx2 %0, %1, off sc0 sc1" :: "v"(p), "v"(v) : "memory");
}
__device__ __forceinline__ f32x4 ld16sc1(const void* p) {
  f32x4 r;
  asm volatile("global_load_dwordx4 %0, %1, off sc0 sc1" : "=v"(r) : "v"(p) : "memory");
  return r;
}
__device__ __forceinline__ void st16u(void* p, u32x4 v) {
  asm volatile("global_store_dwordx4 %0, %1, off sc0 sc1\n\ts_nop 1" :: "v"(p), "v"(v) : "memory");
}
__device__ __forceinline__ void st16(void* p, f32x4 v) {
  asm volatile("global_store_dwordx4 %0, %1, off sc0 sc1\n\ts_nop 1" :: "v"(p), "v"(v) : "memory");
}
__device__ __forceinline__ void glds16(const void* g, unsigned l) {
  unsigned keep;
  l = __builtin_amdgcn_readfirstlane(l);
  asm volatile("s_mov_b32 %0, m0\n\ts_mov_b32 m0, %2\n\ts_nop 0\n\tglobal_load_lds_dwordx4 %1, off\n\ts_mov_b32 m0, %0"
               : "=&s"(keep) : "v"(g), "s"(l) : "memory");
}
__device__ __forceinline__ bf16x4 tr_read(const char* p) {
  return __builtin_amdgcn_ds_read_tr16_b64_v4i16((LDS_AS bf16x4*)p);
}
__device__ __forceinline__ bf16x8 cat8(bf16x4 a, bf16x4 b) {
  return __builtin_shufflevector(a, b, 0, 1, 2, 3, 4, 5, 6, 7);
}
#define KSTART(id)
#define KEND(id)
#define WAIT_V(n) asm volatile("s_waitcnt vmcnt(" #n ")" ::: "memory")
#define BAR() do { asm volatile("s_waitcnt lgkmcnt(0)" ::: "memory"); __builtin_amdgcn_s_barrier(); asm volatile("" ::: "memory"); } while (0)

static constexpr int CV_NB = 48;
static constexpr int CV_KV_BYTES = 8 * CV_NB * 16;
static constexpr int CV_CS8 = 552;
static constexpr int CV_HC_BYTES = 4 * CV_CS8 * 8;

__global__ __launch_bounds__(256) void kvt_kernel(const u16* __restrict__ kin, const u16* __restrict__ vin,
                                                  const u16* __restrict__ shift_k, u16* __restrict__ kvt,
                                                  unsigned* __restrict__ cnt) {
  __shared__ __attribute__((aligned(16))) u16 tl[64][72];
  const int tid = threadIdx.x;
  if (blockIdx.x == 0 && tid < 128) cnt[tid] = 0u;
  KSTART(0);
  const int blk = blockIdx.x & 31, cht = blockIdx.x >> 5;
  const int cg = tid & 7, c0 = cht * 64 + cg * 8;
  float sk[8][4];
#pragma unroll
  for (int e = 0; e < 8; ++e) {
    u32x2 w = *(const u32x2*)(shift_k + (c0 + e) * 4);
    sk[e][0] = lo16(w[0]); sk[e][1] = hi16(w[0]); sk[e][2] = lo16(w[1]); sk[e][3] = hi16(w[1]);
  }
#pragma unroll
  for (int ps = 0; ps < 2; ++ps) {
    const int r = (tid >> 3) + 32 * ps, t = blk * 64 + r;
    u32x4 kr[4];
#pragma unroll
    for (int j = 0; j < 4; ++j) {
      int tt = t - 3 + j;
      if (tt >= 0) kr[j] = *(const u32x4*)(kin + (size_t)tt * D_ + c0);
      else kr[j] = (u32x4){0u, 0u, 0u, 0u};
    }
    u32x4 vr = *(const u32x4*)(vin + (size_t)t * D_ + c0);
    const int slot = (r & ~7) + 7 - (r & 7);
#pragma unroll
    for (int e = 0; e < 8; ++e) {
      float acc = 0.f;
#pragma unroll
      for (int j = 0; j < 4; ++j) {
        float kvv = (e & 1) ? hi16(kr[j][e >> 1]) : lo16(kr[j][e >> 1]);
        acc = rbf(acc + rbf(kvv * sk[e][3 - j]));
      }
      float vv = (e & 1) ? hi16(vr[e >> 1]) : lo16(vr[e >> 1]);
      tl[cg * 8 + e][slot] = f2bf(acc * vv);
    }
  }
  __syncthreads();
#pragma unroll
  for (int ps = 0; ps < 2; ++ps) {
    const int ch = (tid >> 3) + 32 * ps, pl = tid & 7;
    u32x4 w = *(const u32x4*)(&tl[ch][pl * 8]);
    *(u32x4*)(kvt + ((size_t)(cht * 64 + ch) * 32 + blk) * 64 + pl * 8) = w;
  }
  KEND(0);
}

struct ConvFr { bf16x4 G[8]; bf16x8 B[2]; };
__device__ __forceinline__ void conv_load(ConvFr& f, unsigned ha, unsigned ka) {
  asm volatile("ds_read_b64 %0, %10\n\tds_read_b64 %1, %10 offset:8\n\t"
               "ds_read_b64 %2, %10 offset:32\n\tds_read_b64 %3, %10 offset:40\n\t"
               "ds_read_b64 %4, %10 offset:64\n\tds_read_b64 %5, %10 offset:72\n\t"
               "ds_read_b64 %6, %10 offset:96\n\tds_read_b64 %7, %10 offset:104\n\t"
               "ds_read_b128 %8, %11\n\tds_read_b128 %9, %11 offset:3072"
               : "=&v"(f.G[0]), "=&v"(f.G[1]), "=&v"(f.G[2]), "=&v"(f.G[3]), "=&v"(f.G[4]), "=&v"(f.G[5]), "=&v"(f.G[6]), "=&v"(f.G[7]),
                 "=&v"(f.B[0]), "=&v"(f.B[1])
               : "v"(ha), "v"(ka) : "memory");
}
template <int N> __device__ __forceinline__ void conv_wait(ConvFr& f) {
  asm volatile("s_waitcnt lgkmcnt(%10)"
               : "+v"(f.G[0]), "+v"(f.G[1]), "+v"(f.G[2]), "+v"(f.G[3]), "+v"(f.G[4]), "+v"(f.G[5]), "+v"(f.G[6]), "+v"(f.G[7]),
                 "+v"(f.B[0]), "+v"(f.B[1]) : "i"(N) : "memory");
}
__device__ __forceinline__ void conv_mma(const ConvFr& f, bf16x8& Gm2, bf16x8& Gm1, f32x4 (&acc)[4]) {
  bf16x8 G0 = cat8(f.G[0], f.G[1]), G1 = cat8(f.G[2], f.G[3]), G2 = cat8(f.G[4], f.G[5]), G3 = cat8(f.G[6], f.G[7]);
  acc[0] = __builtin_amdgcn_mfma_f32_16x16x32_bf16(G0, f.B[0], acc[0], 0, 0, 0);
  acc[1] = __builtin_amdgcn_mfma_f32_16x16x32_bf16(G1, f.B[0], acc[1], 0, 0, 0);
  acc[2] = __builtin_amdgcn_mfma_f32_16x16x32_bf16(G2, f.B[0], acc[2], 0, 0, 0);
  acc[3] = __builtin_amdgcn_mfma_f32_16x16x32_bf16(G3, f.B[0], acc[3], 0, 0, 0);
  acc[0] = __builtin_amdgcn_mfma_f32_16x16x32_bf16(Gm2, f.B[1], acc[0], 0, 0, 0);
  acc[1] = __builtin_amdgcn_mfma_f32_16x16x32_bf16(Gm1, f.B[1], acc[1], 0, 0, 0);
  acc[2] = __builtin_amdgcn_mfma_f32_16x16x32_bf16(G0, f.B[1], acc[2], 0, 0, 0);
  acc[3] = __builtin_amdgcn_mfma_f32_16x16x32_bf16(G1, f.B[1], acc[3], 0, 0, 0);
  Gm2 = G2; Gm1 = G3;
}
__device__ __forceinline__ void conv_part(const char* hb, const char* kvb, int d0, int d1, f32x4 (&acc)[4]) {
  bf16x8 Gm2, Gm1;
  if (d0 == 0) { Gm2 = (bf16x8){0, 0, 0, 0, 0, 0, 0, 0}; Gm1 = Gm2; }
  else {
    const char* p = hb + (4 * d0 - 2) * 32;
    Gm2 = cat8(*(const bf16x4*)p, *(const bf16x4*)(p + 8));
    Gm1 = cat8(*(const bf16x4*)(p + 32), *(const bf16x4*)(p + 40));
  }
  unsigned ha = lds_addr(hb) + d0 * 128, ka = lds_addr(kvb) - d0 * 16;
  ConvFr fa, fb;
  conv_load(fa, ha, ka);
#pragma unroll 1
  for (int dl = d0; dl < d1; dl += 2) {
    conv_load(fb, ha + 128, ka - 16);
    conv_wait<10>(fa);
    conv_mma(fa, Gm2, Gm1, acc);
    __builtin_amdgcn_sched_barrier(0);
    ha += 256; ka -= 32;
    conv_load(fa, ha, ka);
    conv_wait<10>(fb);
    conv_mma(fb, Gm2, Gm1, acc);
    __builtin_amdgcn_sched_barrier(0);
  }
  conv_wait<0>(fa);
}

__global__ __launch_bounds__(512) void conv_kernel(const u16* __restrict__ kvt, const u16* __restrict__ ssm_k,
                                                   const u16* __restrict__ ssm_D, u16* __restrict__ yout,
                                                   const u16* __restrict__ w1, const u16* __restrict__ w3,
                                                   const u16* __restrict__ w2) {
  if (blockIdx.x >= D_ / 4) {
    const int nb = gridDim.x - D_ / 4, b = blockIdx.x - D_ / 4;
    constexpr int NCH = D_ * F_ / 8;
    u32x4 a = (u32x4){0u, 0u, 0u, 0u};
    for (int i = b * 512 + threadIdx.x; i < NCH; i += nb * 512) {
      a ^= *(const u32x4*)(w1 + (size_t)i * 8);
      a ^= *(const u32x4*)(w3 + (size_t)i * 8);
      a ^= *(const u32x4*)(w2 + (size_t)i * 8);
    }
    if (a[0] == 0x9e3779b9u && a[1] == 0x7f4a7c15u && a[2] == 0x85ebca6bu && a[3] == 0xc2b2ae35u) yout[0] = 0;
    return;
  }
  __shared__ __attribute__((aligned(16))) char lds[4 * CV_KV_BYTES + 4 * CV_HC_BYTES + 16384];
  char* kvs = lds;
  char* hcs = lds + 4 * CV_KV_BYTES;
  char* red = lds + 4 * CV_KV_BYTES + 4 * CV_HC_BYTES;
  const int tid = threadIdx.x, lane = tid & 63, wave = tid >> 6;
  const int d0 = blockIdx.x * 4;
  KSTART(1);
  const int t0 = tid * 4;
#pragma unroll
  for (int ps = 0; ps < 2; ++ps) {
    int it = tid + 512 * ps, c = it >> 8, blk = (it >> 3) & 31, pl = it & 7;
    u32x4 w = *(const u32x4*)(kvt + ((size_t)(d0 + c) * 32 + blk) * 64 + pl * 8);
    *(u32x4*)(kvs + c * CV_KV_BYTES + (pl * CV_NB + blk + 16) * 16) = w;
  }
  for (int it = tid; it < 4 * 264; it += 512) {
    int c = it / 264, qp = it % 264;
    int q = qp - 4;
    const u16* hp = ssm_k + (size_t)(d0 + c) * S_;
    u32x4 a = (u32x4){0u, 0u, 0u, 0u}, b = (u32x4){0u, 0u, 0u, 0u};
    if (q >= 0 && q < 256) a = *(const u32x4*)(hp + q * 8);
    if (q + 1 >= 0 && q + 1 < 256) b = *(const u32x4*)(hp + (q + 1) * 8);
    char* dst = hcs + c * CV_HC_BYTES + qp * 16;
    *(u32x4*)(dst + 0 * CV_CS8 * 8) = a;
    u32x4 s1;
    s1[0] = __builtin_amdgcn_alignbit(a[1], a[0], 16);
    s1[1] = __builtin_amdgcn_alignbit(a[2], a[1], 16);
    s1[2] = __builtin_amdgcn_alignbit(a[3], a[2], 16);
    s1[3] = __builtin_amdgcn_alignbit(b[0], a[3], 16);
    *(u32x4*)(dst + 1 * CV_CS8 * 8) = s1;
    u32x4 s2 = (u32x4){a[1], a[2], a[3], b[0]};
    *(u32x4*)(dst + 2 * CV_CS8 * 8) = s2;
    u32x4 s3;
    s3[0] = s1[1]; s3[1] = s1[2]; s3[2] = s1[3];
    s3[3] = __builtin_amdgcn_alignbit(b[1], b[0], 16);
    *(u32x4*)(dst + 3 * CV_CS8 * 8) = s3;
  }
  {
    int c = tid >> 7, pl = (tid >> 4) & 7, b = tid & 15;
    *(u32x4*)(kvs + c * CV_KV_BYTES + (pl * CV_NB + b) * 16) = (u32x4){0u, 0u, 0u, 0u};
  }
  float dd[4];
#pragma unroll
  for (int c = 0; c < 4; ++c) dd[c] = bf2f(ssm_D[d0 + c]);
  __syncthreads();

  const int ch = wave & 3, part = wave >> 2;
  const int c16 = lane & 15, g = lane >> 4;
  f32x4 accA[4], accB[4];
#pragma unroll
  for (int i = 0; i < 4; ++i) { accA[i] = (f32x4){0, 0, 0, 0}; accB[i] = (f32x4){0, 0, 0, 0}; }
  {
    const char* kvb = kvs + ch * CV_KV_BYTES + (g * CV_NB + 16 + c16) * 16;
    const int r = (c16 + 1) & 3, w = (c16 + 1) >> 2;
    const char* hb = hcs + ch * CV_HC_BYTES + (r * CV_CS8 - 2 * g + 6 + w) * 8;
    if (part == 0) {
      conv_part(hb, kvb + 16 * 16, 0, 24, accA);
    } else {
      conv_part(hb, kvb, 0, 16, accA);
      conv_part(hb, kvb + 16 * 16, 24, 32, accB);
#pragma unroll
      for (int i = 0; i < 4; ++i) *(f32x4*)(red + ((ch * 4 + i) * 64 + lane) * 16) = accB[i];
    }
  }
  __syncthreads();
  {
    char* yt = hcs + ch * 4096;
    const int ct = part ? 0 : 1;
#pragma unroll
    for (int i = 0; i < 4; ++i) {
      f32x4 a = accA[i];
      if (part == 0) a += *(const f32x4*)(red + ((ch * 4 + i) * 64 + lane) * 16);
      int t = 64 * (16 * ct + c16) + 16 * i + 4 * g;
      u32x2 w;
      w[0] = pack2(a[0], a[1]);
      w[1] = pack2(a[2], a[3]);
      *(u32x2*)(yt + t * 2) = w;
    }
  }
  __syncthreads();
  {
    u32x2 yc[4];
    float kvf[4][4];
    const int plane = (t0 & 63) >> 3, blk = (t0 >> 6) + 16, hi = (t0 >> 2) & 1;
#pragma unroll
    for (int c = 0; c < 4; ++c) {
      yc[c] = *(const u32x2*)(hcs + c * 4096 + t0 * 2);
      u32x2 kw = *(const u32x2*)(kvs + c * CV_KV_BYTES + (plane * CV_NB + blk) * 16 + (hi ? 0 : 8));
      kvf[c][3] = lo16(kw[0]); kvf[c][2] = hi16(kw[0]); kvf[c][1] = lo16(kw[1]); kvf[c][0] = hi16(kw[1]);
    }
#pragma unroll
    for (int i = 0; i < 4; ++i) {
      float o[4];
#pragma unroll
      for (int c = 0; c < 4; ++c) {
        unsigned w = yc[c][i >> 1];
        float yv = bf2f((u16)((i & 1) ? (w >> 16) : (w & 0xffff)));
        o[c] = yv + rbf(kvf[c][i] * dd[c]);
      }
      u32x2 w;
      w[0] = pack2(o[0], o[1]);
      w[1] = pack2(o[2], o[3]);
      *(u32x2*)(yout + (size_t)(t0 + i) * D_ + d0) = w;
    }
  }
  KEND(1);
}

template <int CTRL> __device__ __forceinline__ float dpp_add(float v) {
  int o = __builtin_amdgcn_update_dpp(0, __float_as_int(v), CTRL, 0xf, 0xf, true);
  return v + __int_as_float(o);
}
__device__ __forceinline__ float sum64(float v) {
  v = dpp_add<0xB1>(v);
  v = dpp_add<0x4E>(v);
  v = dpp_add<0x141>(v);
  v = dpp_add<0x140>(v);
  int iv = __float_as_int(v);
  float a = __int_as_float(__builtin_amdgcn_readlane(iv, 0)), b = __int_as_float(__builtin_amdgcn_readlane(iv, 16));
  float c = __int_as_float(__builtin_amdgcn_readlane(iv, 32)), d = __int_as_float(__builtin_amdgcn_readlane(iv, 48));
  return (a + b) + (c + d);
}
__device__ __forceinline__ void layernorm9(const float (&x)[9], float (&o)[9]) {
  float s = 0.f;
#pragma unroll
  for (int i = 0; i < 9; ++i) s += x[i];
  s = sum64(s);
  const float mean = s * (1.0f / D_);
  float q = 0.f;
#pragma unroll
  for (int i = 0; i < 9; ++i) { float d = x[i] - mean; q += d * d; }
  q = sum64(q);
  const float var = q * (1.0f / D_);
  const float mb = rbf(mean);
  const float vb = rbf(var);
  const float sd = rbf(sqrtf(rbf(vb + 1e-5f)));
#pragma unroll
  for (int i = 0; i < 9; ++i) o[i] = rbf(rbf(x[i] - mb) / sd);
}
__device__ __forceinline__ void load9(const u16* __restrict__ p, int l, float (&x)[9]) {
  u32x2 a = *(const u32x2*)(p + 4 * l), b = *(const u32x2*)(p + 256 + 4 * l);
  u16 c = p[512 + l];
  x[0] = lo16(a[0]); x[1] = hi16(a[0]); x[2] = lo16(a[1]); x[3] = hi16(a[1]);
  x[4] = lo16(b[0]); x[5] = hi16(b[0]); x[6] = lo16(b[1]); x[7] = hi16(b[1]);
  x[8] = bf2f(c);
}

template <int MODE, int KID>
__global__ __launch_bounds__(256) void row_kernel(const u16* __restrict__ xin, const float* __restrict__ part,
                                                  const u16* __restrict__ y, u16* __restrict__ xout,
                                                  u16* __restrict__ xn_t) {
  KSTART(KID);
  const int l = threadIdx.x & 63;
  const int row = blockIdx.x * 4 + (threadIdx.x >> 6);
  float x[9], yv[9];
  load9(xin + (size_t)row * D_, l, x);
  if (MODE <= 1) load9(y + (size_t)row * D_, l, yv);
  if (MODE >= 1) {
    f32x4 a = (f32x4){0, 0, 0, 0}, b = (f32x4){0, 0, 0, 0};
    float c = 0.f;
#pragma unroll
    for (int s = 0; s < NSPLIT; ++s) {
      const float* pp = part + ((size_t)s * S_ + row) * D_;
      a += *(const f32x4*)(pp + 4 * l);
      b += *(const f32x4*)(pp + 256 + 4 * l);
      c += pp[512 + l];
    }
#pragma unroll
    for (int e = 0; e < 4; ++e) { x[e] = rbf(x[e] + rbf(a[e])); x[4 + e] = rbf(x[4 + e] + rbf(b[e])); }
    x[8] = rbf(x[8] + rbf(c));
  }
  if (MODE <= 1) {
    float n[9];
    layernorm9(x, n);
#pragma unroll
    for (int i = 0; i < 9; ++i) x[i] = rbf(x[i] + rbf(n[i] * yv[i]));
  }
  {
    u16* po = xout + (size_t)row * D_;
    u32x2 w;
    w[0] = cvt2(x[0], x[1]); w[1] = cvt2(x[2], x[3]);
    *(u32x2*)(po + 4 * l) = w;
    w[0] = cvt2(x[4], x[5]); w[1] = cvt2(x[6], x[7]);
    *(u32x2*)(po + 256 + 4 * l) = w;
    po[512 + l] = f2bf(x[8]);
  }
  if (MODE <= 1) {
    float n[9];
    layernorm9(x, n);
    const int tt = row >> 4, c = row & 15;
    char* base = (char*)xn_t + (size_t)tt * KT1 * 1024 + c * 16;
    u32x2 w;
    w[0] = cvt2(n[0], n[1]); w[1] = cvt2(n[2], n[3]);
    *(u32x2*)(base + (l >> 3) * 1024 + (l & 3) * 256 + ((l >> 2) & 1) * 8) = w;
    w[0] = cvt2(n[4], n[5]); w[1] = cvt2(n[6], n[7]);
    *(u32x2*)(base + (8 + (l >> 3)) * 1024 + (l & 3) * 256 + ((l >> 2) & 1) * 8) = w;
    *(u16*)(base + (16 + (l >> 5)) * 1024 + ((l >> 2) & 3) * 256 + ((l >> 4) & 1) * 8 + (l & 3) * 2) = f2bf(n[8]);
  }
  KEND(KID);
}

static constexpr int STG = 34816;
static constexpr int NSTG = 4;

__device__ __forceinline__ u32x2 silu_mul4(f32x4 g, f32x4 u) {
  u32x2 o;
#pragma unroll
  for (int i = 0; i < 2; ++i) {
    unsigned gw = cvt2(g[2 * i], g[2 * i + 1]);
    unsigned uw = cvt2(u[2 * i], u[2 * i + 1]);
    float g0 = lo16(gw), g1 = hi16(gw);
    float s0 = g0 * __builtin_amdgcn_rcpf(1.0f + __builtin_amdgcn_exp2f(g0 * -1.44269504089f));
    float s1 = g1 * __builtin_amdgcn_rcpf(1.0f + __builtin_amdgcn_exp2f(g1 * -1.44269504089f));
    unsigned sw = cvt2(s0, s1);
    o[i] = cvt2(lo16(sw) * lo16(uw), hi16(sw) * hi16(uw));
  }
  return o;
}

template <int N> __device__ __forceinline__ void wait_vm2(bf16x8& a, bf16x8& b) {
  asm volatile("s_waitcnt vmcnt(%2)" : "+v"(a), "+v"(b) : "i"(N) : "memory");
}
__device__ __forceinline__ void wait_vm2_dyn(int n, bf16x8& a, bf16x8& b) {
  switch (n) {
    case 0: wait_vm2<0>(a, b); break;
    case 1: wait_vm2<1>(a, b); break;
    case 2: wait_vm2<2>(a, b); break;
    case 3: wait_vm2<3>(a, b); break;
    case 4: wait_vm2<4>(a, b); break;
    case 5: wait_vm2<5>(a, b); break;
    case 6: wait_vm2<6>(a, b); break;
    case 7: wait_vm2<7>(a, b); break;
    case 8: wait_vm2<8>(a, b); break;
    case 9: wait_vm2<9>(a, b); break;
    case 10: wait_vm2<10>(a, b); break;
    case 11: wait_vm2<11>(a, b); break;
    case 12: wait_vm2<12>(a, b); break;
    case 13: wait_vm2<13>(a, b); break;
    case 14: wait_vm2<14>(a, b); break;
    case 15: wait_vm2<15>(a, b); break;
    case 16: wait_vm2<16>(a, b); break;
    case 17: wait_vm2<17>(a, b); break;
    case 18: wait_vm2<18>(a, b); break;
    case 19: wait_vm2<19>(a, b); break;
    case 20: wait_vm2<20>(a, b); break;
    case 21: wait_vm2<21>(a, b); break;
    case 22: wait_vm2<22>(a, b); break;
    case 23: wait_vm2<23>(a, b); break;
    case 24: wait_vm2<24>(a, b); break;
    case 25: wait_vm2<25>(a, b); break;
    case 26: wait_vm2<26>(a, b); break;
    case 27: wait_vm2<27>(a, b); break;
    case 28: wait_vm2<28>(a, b); break;
    case 29: wait_vm2<29>(a, b); break;
    case 30: wait_vm2<30>(a, b); break;
    case 31: wait_vm2<31>(a, b); break;
    default: wait_vm2<0>(a, b); break;
  }
}
template <int N> __device__ __forceinline__ void wait_vm() {
  asm volatile("s_waitcnt vmcnt(%0)" :: "i"(N) : "memory");
}
__device__ __forceinline__ bf16x8 gload16(const void* p) {
  bf16x8 r;
  asm volatile("global_load_dwordx4 %0, %1, off" : "=v"(r) : "v"(p) : "memory");
  return r;
}
__device__ __forceinline__ u32x2 swiglu4(f32x4 g, f32x4 u) {
  f32x4 e;
#pragma unroll
  for (int i = 0; i < 4; ++i) e[i] = __builtin_amdgcn_exp2f(g[i] * -1.44269504089f);
  e = e + 1.0f;
#pragma unroll
  for (int i = 0; i < 4; ++i) e[i] = __builtin_amdgcn_rcpf(e[i]);
  f32x4 h = (g * u) * e;
  u32x2 o;
  o[0] = cvt2(h[0], h[1]);
  o[1] = cvt2(h[2], h[3]);
  return o;
}

template <int KID>
__global__ __launch_bounds__(512, 2) void gemm1_kernel(const u16* __restrict__ xn_t, const u16* __restrict__ w1,
                                                       const u16* __restrict__ w3, u16* __restrict__ h_t) {
  __shared__ __attribute__((aligned(16))) char lds[NSTG * STG];
  const int tid = threadIdx.x, lane = tid & 63;
  const int wave = __builtin_amdgcn_readfirstlane(tid >> 6);
  const int wm = wave & 3, wn = wave >> 2;
  const int bid = blockIdx.x, xcd = bid & 7, idx = bid >> 3;
  const int ft = xcd * 4 + (idx & 3), mt = idx >> 2;
  KSTART(KID);
  const char* xs = (const char*)xn_t + ((size_t)(mt * 16 + 2 * wave) * KT1) * 1024 + lane * 16;
  const char *ws0, *ws1, *ws2;
  {
    const int colA = 32 * (9 * (ft >> 1) + 4 * (ft & 1)), colB = 32 * (9 * (ft >> 1) + 8) + 16 * (ft & 1) - 128;
    int ci = wave * 128 + lane;
    int img = ci / 576, rem = ci % 576, row = rem / 18, cc = rem % 18;
    ws0 = (const char*)((img ? w3 : w1) + (size_t)row * F_ + (cc < 16 ? colA : colB) + cc * 8);
    ci += 64;
    img = ci / 576; rem = ci % 576; row = rem / 18; cc = rem % 18;
    ws1 = (const char*)((img ? w3 : w1) + (size_t)row * F_ + (cc < 16 ? colA : colB) + cc * 8);
    ci = 1024 + wave * 16 + (lane & 15);
    img = ci / 576; rem = ci % 576; row = rem / 18; cc = rem % 18;
    ws2 = (const char*)((img ? w3 : w1) + (size_t)row * F_ + (cc < 16 ? colA : colB) + cc * 8);
  }
  constexpr size_t WSTEP = (size_t)32 * F_ * 2;
  const unsigned lbase = lds_addr(lds);
  auto issue = [&](int s) {
    unsigned sb = lbase + (s & 3) * STG;
    glds16(xs + s * 1024, sb + (2 * wave) * 1024);
    glds16(xs + KT1 * 1024 + s * 1024, sb + (2 * wave + 1) * 1024);
    glds16(ws0 + s * WSTEP, sb + 16384 + wave * 2048);
    glds16(ws1 + s * WSTEP, sb + 16384 + wave * 2048 + 1024);
    if (lane < 16) glds16(ws2 + s * WSTEP, sb + 16384 + 16384 + wave * 256);
  };

  f32x4 acc[9][4];
#pragma unroll
  for (int n = 0; n < 9; ++n)
#pragma unroll
    for (int m = 0; m < 4; ++m) acc[n][m] = (f32x4){0, 0, 0, 0};

  const int g = lane >> 4, q = (lane & 15) >> 2, p = lane & 3;
  const int cb = wn * 64, cm = 128 + 8 * wn;
  const int xoff = wm * 4096 + lane * 16;
  const int woff = 16384 + (4 * g + q) * 288 + (cb + 4 * p) * 2;
  const int moff = 16384 + (p >> 1) * 9216 + (4 * g + q) * 288 + (cm + 4 * (p & 1)) * 2;

  issue(0); issue(1); issue(2);
  auto wread = [&](const char* sb, int n) -> bf16x8 {
    const char* wp = (n < 8) ? (sb + woff + (n >> 2) * 9216 + (n & 3) * 32) : (sb + moff);
    return cat8(tr_read(wp), tr_read(wp + 16 * 288));
  };
  bf16x8 xa[4], xb[4], fa, fb, fc;
  WAIT_V(10);
  __builtin_amdgcn_s_barrier();
  asm volatile("" ::: "memory");
#pragma unroll
  for (int m = 0; m < 4; ++m) xa[m] = *(const bf16x8*)(lds + xoff + m * 1024);
  fa = wread(lds, 0);
  fb = wread(lds, 1);

#define SB0 __builtin_amdgcn_sched_barrier(0)
#define MM(n, w, x) _Pragma("unroll") for (int m = 0; m < 4; ++m) acc[n][m] = __builtin_amdgcn_mfma_f32_16x16x32_bf16(w, x[m], acc[n][m], 0, 0, 0)
  auto body = [&](int s, bf16x8 (&xc)[4], bf16x8 (&xn)[4], bool last) {
    const char* sb = lds + (s & 3) * STG;
    const char* sn = lds + ((s + 1) & 3) * STG;
    fc = wread(sb, 2); SB0;
    MM(0, fa, xc); SB0; fa = wread(sb, 3); SB0;
    MM(1, fb, xc); SB0; fb = wread(sb, 4); SB0;
    MM(2, fc, xc); SB0; fc = wread(sb, 5); SB0;
    MM(3, fa, xc); SB0; fa = wread(sb, 6); SB0;
    MM(4, fb, xc); SB0; fb = wread(sb, 7); SB0;
    MM(5, fc, xc); SB0; fc = wread(sb, 8); SB0;
    if (!last) {
      if (s < KT1 - 2) WAIT_V(5); else WAIT_V(0);
      __builtin_amdgcn_s_barrier();
      asm volatile("" ::: "memory");
#pragma unroll
      for (int m = 0; m < 4; ++m) xn[m] = *(const bf16x8*)(sn + xoff + m * 1024);
      SB0;
    }
    MM(6, fa, xc); SB0; if (!last) fa = wread(sn, 0); SB0;
    if (!last) { if (s + 3 < KT1) issue(s + 3); }
    SB0;
    MM(7, fb, xc); SB0; if (!last) fb = wread(sn, 1); SB0;
    MM(8, fc, xc); SB0;
  };
  for (int s = 0; s < KT1 - 2; s += 2) {
    body(s, xa, xb, false);
    body(s + 1, xb, xa, false);
  }
  body(KT1 - 2, xa, xb, false);
  body(KT1 - 1, xb, xa, true);
#undef MM

  const int c = lane & 15;
  const int P0 = 9 * (ft >> 1) + 4 * (ft & 1) + 2 * wn;
  const int PX = 9 * (ft >> 1) + 8;
#pragma unroll
  for (int m = 0; m < 4; ++m) {
    const int tt = mt * 16 + wm * 4 + m;
    char* hb = (char*)h_t + (size_t)tt * KT2 * 1024;
#pragma unroll
    for (int n = 0; n < 2; ++n) {
      u32x2 a = swiglu4(acc[2 * n][m], acc[4 + 2 * n][m]);
      u32x2 b = swiglu4(acc[2 * n + 1][m], acc[5 + 2 * n][m]);
      st16u(hb + (size_t)(P0 + n) * 1024 + lane * 16, (u32x4){a[0], a[1], b[0], b[1]});
    }
    {
      f32x4 o;
#pragma unroll
      for (int r = 0; r < 4; ++r) o[r] = __shfl_xor(acc[8][m][r], 32, 64);
      if (lane < 32) {
        u32x2 w = swiglu4(acc[8][m], o);
        int gs = 2 * wn + g;
        *(u32x2*)(hb + (size_t)PX * 1024 + (gs * 16 + c) * 16 + (ft & 1) * 8) = w;
      }
    }
  }
  KEND(KID);
}

static constexpr int WST = 18432;
template <int KID>
__global__ __launch_bounds__(512, 2) void gemm2_kernel(const u16* __restrict__ h_t, const u16* __restrict__ w2,
                                                       float* __restrict__ part, unsigned* __restrict__ cnt,
                                                       const u16* __restrict__ xin, u16* __restrict__ xout) {
  __shared__ __attribute__((aligned(16))) char lds[8 * WST];
  const int tid = threadIdx.x, lane = tid & 63;
  const int wave = __builtin_amdgcn_readfirstlane(tid >> 6);
  const int wm = wave & 3, wk = wave >> 2;
  const int bid = blockIdx.x, xcd = bid & 7, idx = bid >> 3;
  const int ks = xcd >> 1;
  const int nt = idx & 3, mt = (xcd & 1) * 8 + (idx >> 2);
  const int kt0 = ks * 36;
  KSTART(KID);

  const char* xs0 = (const char*)h_t + ((size_t)(mt * 8 + wm * 2) * KT2 + kt0 + wk * 18) * 1024 + lane * 16;
  const char* xs1 = xs0 + (size_t)KT2 * 1024;
  const char *ws0, *ws1, *ws2;
  {
    int ci = wave * 128 + lane;
    int img = ci / 576, rem = ci % 576, row = rem / 18, cc = rem % 18;
    ws0 = (const char*)(w2 + (size_t)((kt0 + img * 18) * 32 + row) * D_ + nt * 144 + cc * 8);
    ci += 64;
    img = ci / 576; rem = ci % 576; row = rem / 18; cc = rem % 18;
    ws1 = (const char*)(w2 + (size_t)((kt0 + img * 18) * 32 + row) * D_ + nt * 144 + cc * 8);
    ci = 1024 + wave * 16 + (lane & 15);
    img = ci / 576; rem = ci % 576; row = rem / 18; cc = rem % 18;
    ws2 = (const char*)(w2 + (size_t)((kt0 + img * 18) * 32 + row) * D_ + nt * 144 + cc * 8);
  }
  constexpr size_t WSTEP = (size_t)32 * D_ * 2;
  const unsigned lbase = lds_addr(lds);
  auto issueW = [&](int s) {
    unsigned sb = lbase + (s & 7) * WST;
    glds16(ws0, sb + wave * 2048);
    glds16(ws1, sb + wave * 2048 + 1024);
    if (lane < 16) glds16(ws2, sb + 16384 + wave * 256);
    ws0 += WSTEP; ws1 += WSTEP; ws2 += WSTEP;
    asm volatile("" : "+v"(ws0), "+v"(ws1), "+v"(ws2));
  };
  auto loadX = [&](bf16x8 (&x)[2]) {
    x[0] = gload16(xs0);
    x[1] = gload16(xs1);
    xs0 += 1024; xs1 += 1024;
    asm volatile("" : "+v"(xs0), "+v"(xs1));
  };

  f32x4 acc[9][2];
#pragma unroll
  for (int n = 0; n < 9; ++n) { acc[n][0] = (f32x4){0, 0, 0, 0}; acc[n][1] = (f32x4){0, 0, 0, 0}; }

  const int g = lane >> 4, q = (lane & 15) >> 2, p = lane & 3;
  const int woff = wk * 9216 + (4 * g + q) * 288 + (4 * p) * 2;

  bf16x8 xr[6][2];
#pragma unroll
  for (int j = 0; j < 5; ++j) { loadX(xr[j]); issueW(j); }
  issueW(5); issueW(6);
  wait_vm2<3 + 4 * 5 + 6>(xr[0][0], xr[0][1]);
  __builtin_amdgcn_s_barrier();
  asm volatile("" ::: "memory");

  auto wread = [&](const char* sb, int n) -> bf16x8 {
    const char* wp = sb + n * 32;
    return cat8(tr_read(wp), tr_read(wp + 16 * 288));
  };
#define SB0 __builtin_amdgcn_sched_barrier(0)
#define MM(n, w, x) do { acc[n][0] = __builtin_amdgcn_mfma_f32_16x16x32_bf16(w, x[0], acc[n][0], 0, 0, 0); \
                         acc[n][1] = __builtin_amdgcn_mfma_f32_16x16x32_bf16(w, x[1], acc[n][1], 0, 0, 0); } while (0)
  bf16x8 fa = wread(lds + woff, 0), fb = wread(lds + woff, 1), fc;
  auto stage = [&](int s, bf16x8 (&xc)[2], bf16x8 (&xn)[2], bf16x8 (&xp)[2], bool last) {
    const char* sb = lds + (s & 7) * WST + woff;
    const char* sn = lds + ((s + 1) & 7) * WST + woff;
    fc = wread(sb, 2); SB0;
    MM(0, fa, xc); fa = wread(sb, 3); SB0;
    MM(1, fb, xc); fb = wread(sb, 4); SB0;
    MM(2, fc, xc); fc = wread(sb, 5); SB0;
    MM(3, fa, xc); fa = wread(sb, 6); SB0;
    MM(4, fb, xc); fb = wread(sb, 7); SB0;
    MM(5, fc, xc); fc = wread(sb, 8); SB0;
    if (!last) {
      if (s <= 11) wait_vm<18>();
      else if (s == 12) wait_vm<15>();
      else if (s == 13) wait_vm<12>();
      else if (s == 14) wait_vm<7>();
      else if (s == 15) wait_vm<2>();
      else wait_vm<0>();
      asm volatile("" : "+v"(xn[0]), "+v"(xn[1]));
      __builtin_amdgcn_s_barrier();
      asm volatile("" ::: "memory");
      SB0;
    }
    MM(6, fa, xc); if (!last) fa = wread(sn, 0); SB0;
    if (s + 5 < 18) loadX(xp);
    if (s + 7 < 18) issueW(s + 7);
    SB0;
    MM(7, fb, xc); if (!last) fb = wread(sn, 1); SB0;
    MM(8, fc, xc); SB0;
  };
#pragma unroll 1
  for (int s = 0; s < 18; s += 6) {
    stage(s + 0, xr[0], xr[1], xr[5], false);
    stage(s + 1, xr[1], xr[2], xr[0], false);
    stage(s + 2, xr[2], xr[3], xr[1], false);
    stage(s + 3, xr[3], xr[4], xr[2], false);
    stage(s + 4, xr[4], xr[5], xr[3], false);
    stage(s + 5, xr[5], xr[0], xr[4], s + 5 == 17);
  }
#undef MM
  BAR();
  f32x4* red = (f32x4*)lds;
  int tz = threadIdx.x; asm volatile("" : "+v"(tz));
  const int ln = tz & 63;
#pragma unroll
  for (int n = 0; n < 9; ++n) {
    if ((n < 5) == (wk == 1)) {
#pragma unroll
      for (int m = 0; m < 2; ++m) red[(wm * 18 + n * 2 + m) * 64 + ln] = acc[n][m];
    }
  }
  BAR();
  const int tile = mt * 4 + nt;
  char* xb = (char*)part + (size_t)tile * (16 * 18432) + ln * 16;
#pragma unroll
  for (int n = 0; n < 9; ++n) {
    if ((n < 5) == (wk == 0)) {
#pragma unroll
      for (int m = 0; m < 2; ++m) {
        acc[n][m] += red[(wm * 18 + n * 2 + m) * 64 + ln];
        if (wm != ks) st16(xb + (size_t)(ks * 4 + wm) * 18432 + (n * 2 + m) * 1024, acc[n][m]);
      }
    }
  }
  asm volatile("s_waitcnt vmcnt(0)" ::: "memory");
  __syncthreads();
  if (tz == 0) {
    __hip_atomic_fetch_add(&cnt[tile], 1u, __ATOMIC_RELAXED, __HIP_MEMORY_SCOPE_AGENT);
    unsigned spins = 0;
    while (__hip_atomic_load(&cnt[tile], __ATOMIC_RELAXED, __HIP_MEMORY_SCOPE_AGENT) < 4u && ++spins < 2000000u)
      __builtin_amdgcn_s_sleep(1);
  }
  __syncthreads();
  if (wm == ks) {
    const int c = ln & 15;
#pragma unroll
    for (int n = 0; n < 9; ++n) {
      if ((n < 5) == (wk == 0)) {
        f32x4 o[2][3];
#pragma unroll
        for (int m = 0; m < 2; ++m)
#pragma unroll
          for (int j = 0; j < 3; ++j) {
            const int src = (ks + 1 + j) & 3;
            o[m][j] = ld16sc1(xb + (size_t)(src * 4 + wm) * 18432 + (n * 2 + m) * 1024);
          }
        asm volatile("s_waitcnt vmcnt(0)" : "+v"(o[0][0]), "+v"(o[0][1]), "+v"(o[0][2]), "+v"(o[1][0]), "+v"(o[1][1]), "+v"(o[1][2]) :: "memory");
#pragma unroll
        for (int m = 0; m < 2; ++m) {
          f32x4 t[4];
#pragma unroll
          for (int j = 0; j < 3; ++j) t[(j + 1)] = o[m][j];
          t[0] = acc[n][m];
          f32x4 sum = t[(0 - ks) & 3];
          sum += t[(1 - ks) & 3]; sum += t[(2 - ks) & 3]; sum += t[(3 - ks) & 3];
          const int token = mt * 128 + wm * 32 + m * 16 + c;
          const size_t off = (size_t)token * D_ + nt * 144 + n * 16 + 4 * (ln >> 4);
          u32x2 xw = *(const u32x2*)(xin + off);
          unsigned s0 = cvt2(sum[0], sum[1]), s1 = cvt2(sum[2], sum[3]);
          u32x2 w;
          w[0] = cvt2(lo16(xw[0]) + lo16(s0), hi16(xw[0]) + hi16(s0));
          w[1] = cvt2(lo16(xw[1]) + lo16(s1), hi16(xw[1]) + hi16(s1));
          *(u32x2*)(xout + off) = w;
        }
      }
    }
  }
  KEND(KID);
}

extern "C" void kernel_launch(void* const* d_in, const int* in_sizes, int n_in,
                              void* d_out, int out_size, void* d_ws, size_t ws_size,
                              hipStream_t stream) {
  const u16* A_q = (const u16*)d_in[0];
  const u16* A_k = (const u16*)d_in[1];
  const u16* A_v = (const u16*)d_in[2];
  const u16* A_shift = (const u16*)d_in[3];
  const u16* A_ssmk = (const u16*)d_in[4];
  const u16* A_ssmD = (const u16*)d_in[5];
  const u16* B_w1 = (const u16*)d_in[7];
  const u16* B_w3 = (const u16*)d_in[8];
  const u16* B_w2 = (const u16*)d_in[9];
  u16* out = (u16*)d_out;

  char* ws = (char*)d_ws;
  constexpr size_t XB = (size_t)S_ * D_ * 2;
  u16* y = (u16*)(ws);
  u16* xa = (u16*)(ws + XB);
  u16* xb = (u16*)(ws + 2 * XB);
  u16* xn = (u16*)(ws + 3 * XB);
  u16* h = (u16*)(ws + 4 * XB);
  float* part = (float*)(ws + 4 * XB + (size_t)S_ * F_ * 2);

  unsigned* cnt = (unsigned*)(ws + 4 * XB + 2 * (size_t)S_ * F_ * 2);
  kvt_kernel<<<288, 256, 0, stream>>>(A_k, A_v, A_shift, xb, cnt);
  conv_kernel<<<256, 512, 0, stream>>>(xb, A_ssmk, A_ssmD, y, B_w1, B_w3, B_w2);
  row_kernel<0, 2><<<S_ / 4, 256, 0, stream>>>(A_q, nullptr, y, xa, xn);
  gemm1_kernel<3><<<256, 512, 0, stream>>>(xn, B_w1, B_w3, h);
  gemm2_kernel<4><<<256, 512, 0, stream>>>(h, B_w2, part, cnt, xa, xb);
  row_kernel<0, 5><<<S_ / 4, 256, 0, stream>>>(xb, nullptr, y, xa, xn);
  gemm1_kernel<6><<<256, 512, 0, stream>>>(xn, B_w1, B_w3, h);
  gemm2_kernel<7><<<256, 512, 0, stream>>>(h, B_w2, part, cnt + 64, xa, out);
}
```

```cpp
#include <hip/hip_runtime.h>
#include <hip/hip_bf16.h>
#include <stdint.h>
#define ST_SC1 1
#define G1MAP 0

typedef __attribute__((ext_vector_type(8))) short bf16x8;
typedef __attribute__((ext_vector_type(4))) short bf16x4;
typedef __attribute__((ext_vector_type(4))) float f32x4;
typedef __attribute__((ext_vector_type(4))) unsigned u32x4;
typedef __attribute__((ext_vector_type(2))) unsigned u32x2;
typedef unsigned short u16;

static constexpr int S_ = 2048, D_ = 576, F_ = 4608;
static constexpr int KT1 = D_ / 32;
static constexpr int KT2 = F_ / 32;
static constexpr int NSPLIT = 4;

#define LDS_AS __attribute__((address_space(3)))
#define GLB_AS __attribute__((address_space(1)))

__device__ __forceinline__ float bf2f(u16 v) { return __uint_as_float(((unsigned)v) << 16); }
__device__ __forceinline__ u16 f2bf(float f) {
  __hip_bfloat16 b = __float2bfloat16(f);
  return *reinterpret_cast<u16*>(&b);
}
__device__ __forceinline__ float rbf(float f) { return bf2f(f2bf(f)); }
__device__ __forceinline__ unsigned pack2(float lo, float hi) {
  return (unsigned)f2bf(lo) | ((unsigned)f2bf(hi) << 16);
}

typedef __attribute__((ext_vector_type(2))) float f32x2;
typedef __attribute__((ext_vector_type(2))) __bf16 xbf16x2;
__device__ __forceinline__ unsigned cvt2(float lo, float hi) {
  f32x2 v = {lo, hi};
  xbf16x2 b = __builtin_convertvector(v, xbf16x2);
  return __builtin_bit_cast(unsigned, b);
}
__device__ __forceinline__ float lo16(unsigned w) { return __uint_as_float(w << 16); }
__device__ __forceinline__ float hi16(unsigned w) { return __uint_as_float(w & 0xffff0000u); }
__device__ __forceinline__ unsigned lds_addr(const void* l) { return (unsigned)(size_t)(LDS_AS const char*)l; }
__device__ __forceinline__ void st8(void* p, u32x2 v) {
  asm volatile("global_store_dwordx2 %0, %1, off sc0 sc1" :: "v"(p), "v"(v) : "memory");
}
__device__ __forceinline__ void st16u(void* p, u32x4 v) {
  asm volatile("global_store_dwordx4 %0, %1, off sc0 sc1\n\ts_nop 1" :: "v"(p), "v"(v) : "memory");
}
__device__ __forceinline__ void st16(void* p, f32x4 v) {
  asm volatile("global_store_dwordx4 %0, %1, off sc0 sc1\n\ts_nop 1" :: "v"(p), "v"(v) : "memory");
}
__device__ __forceinline__ void glds16(const void* g, unsigned l) {
  unsigned keep;
  l = __builtin_amdgcn_readfirstlane(l);
  asm volatile("s_mov_b32 %0, m0\n\ts_mov_b32 m0, %2\n\ts_nop 0\n\tglobal_load_lds_dwordx4 %1, off\n\ts_mov_b32 m0, %0"
               : "=&s"(keep) : "v"(g), "s"(l) : "memory");
}
__device__ __forceinline__ bf16x4 tr_read(const char* p) {
  return __builtin_amdgcn_ds_read_tr16_b64_v4i16((LDS_AS bf16x4*)p);
}
__device__ __forceinline__ bf16x8 cat8(bf16x4 a, bf16x4 b) {
  return __builtin_shufflevector(a, b, 0, 1, 2, 3, 4, 5, 6, 7);
}
#define KSTART(id)
#define KEND(id)
#define KIDV(x) 0
#define WAIT_V(n) asm volatile("s_waitcnt vmcnt(" #n ")" ::: "memory")
#define BAR() do { asm volatile("s_waitcnt lgkmcnt(0)" ::: "memory"); __builtin_amdgcn_s_barrier(); asm volatile("" ::: "memory"); } while (0)

static constexpr int CV_NB = 48;
static constexpr int CV_KV_BYTES = 8 * CV_NB * 16;
static constexpr int CV_CS8 = 552;
static constexpr int CV_HC_BYTES = 4 * CV_CS8 * 8;

__global__ __launch_bounds__(256) void kvt_kernel(const u16* __restrict__ kin, const u16* __restrict__ vin,
                                                  const u16* __restrict__ shift_k, u16* __restrict__ kvt) {
  __shared__ __attribute__((aligned(16))) u16 tl[64][72];
  const int tid = threadIdx.x;
  KSTART(0);
  const int blk = blockIdx.x & 31, cht = blockIdx.x >> 5;
  const int cg = tid & 7, c0 = cht * 64 + cg * 8;
  float sk[8][4];
#pragma unroll
  for (int e = 0; e < 8; ++e) {
    u32x2 w = *(const u32x2*)(shift_k + (c0 + e) * 4);
    sk[e][0] = lo16(w[0]); sk[e][1] = hi16(w[0]); sk[e][2] = lo16(w[1]); sk[e][3] = hi16(w[1]);
  }
#pragma unroll
  for (int ps = 0; ps < 2; ++ps) {
    const int r = (tid >> 3) + 32 * ps, t = blk * 64 + r;
    u32x4 kr[4];
#pragma unroll
    for (int j = 0; j < 4; ++j) {
      int tt = t - 3 + j;
      if (tt >= 0) kr[j] = *(const u32x4*)(kin + (size_t)tt * D_ + c0);
      else kr[j] = (u32x4){0u, 0u, 0u, 0u};
    }
    u32x4 vr = *(const u32x4*)(vin + (size_t)t * D_ + c0);
    const int slot = (r & ~7) + 7 - (r & 7);
#pragma unroll
    for (int e = 0; e < 8; ++e) {
      float acc = 0.f;
#pragma unroll
      for (int j = 0; j < 4; ++j) {
        float kvv = (e & 1) ? hi16(kr[j][e >> 1]) : lo16(kr[j][e >> 1]);
        acc = rbf(acc + rbf(kvv * sk[e][3 - j]));
      }
      float vv = (e & 1) ? hi16(vr[e >> 1]) : lo16(vr[e >> 1]);
      tl[cg * 8 + e][slot] = f2bf(acc * vv);
    }
  }
  __syncthreads();
#pragma unroll
  for (int ps = 0; ps < 2; ++ps) {
    const int ch = (tid >> 3) + 32 * ps, pl = tid & 7;
    u32x4 w = *(const u32x4*)(&tl[ch][pl * 8]);
    *(u32x4*)(kvt + ((size_t)(cht * 64 + ch) * 32 + blk) * 64 + pl * 8) = w;
  }
  KEND(0);
}

struct ConvFr { bf16x4 G[8]; bf16x8 B[2]; };
__device__ __forceinline__ void conv_load(ConvFr& f, unsigned ha, unsigned ka) {
  asm volatile("ds_read_b64 %0, %10\n\tds_read_b64 %1, %10 offset:8\n\t"
               "ds_read_b64 %2, %10 offset:32\n\tds_read_b64 %3, %10 offset:40\n\t"
               "ds_read_b64 %4, %10 offset:64\n\tds_read_b64 %5, %10 offset:72\n\t"
               "ds_read_b64 %6, %10 offset:96\n\tds_read_b64 %7, %10 offset:104\n\t"
               "ds_read_b128 %8, %11\n\tds_read_b128 %9, %11 offset:3072"
               : "=&v"(f.G[0]), "=&v"(f.G[1]), "=&v"(f.G[2]), "=&v"(f.G[3]), "=&v"(f.G[4]), "=&v"(f.G[5]), "=&v"(f.G[6]), "=&v"(f.G[7]),
                 "=&v"(f.B[0]), "=&v"(f.B[1])
               : "v"(ha), "v"(ka) : "memory");
}
template <int N> __device__ __forceinline__ void conv_wait(ConvFr& f) {
  asm volatile("s_waitcnt lgkmcnt(%10)"
               : "+v"(f.G[0]), "+v"(f.G[1]), "+v"(f.G[2]), "+v"(f.G[3]), "+v"(f.G[4]), "+v"(f.G[5]), "+v"(f.G[6]), "+v"(f.G[7]),
                 "+v"(f.B[0]), "+v"(f.B[1]) : "i"(N) : "memory");
}
__device__ __forceinline__ void conv_mma(const ConvFr& f, bf16x8& Gm2, bf16x8& Gm1, f32x4 (&acc)[4]) {
  bf16x8 G0 = cat8(f.G[0], f.G[1]), G1 = cat8(f.G[2], f.G[3]), G2 = cat8(f.G[4], f.G[5]), G3 = cat8(f.G[6], f.G[7]);
  acc[0] = __builtin_amdgcn_mfma_f32_16x16x32_bf16(G0, f.B[0], acc[0], 0, 0, 0);
  acc[1] = __builtin_amdgcn_mfma_f32_16x16x32_bf16(G1, f.B[0], acc[1], 0, 0, 0);
  acc[2] = __builtin_amdgcn_mfma_f32_16x16x32_bf16(G2, f.B[0], acc[2], 0, 0, 0);
  acc[3] = __builtin_amdgcn_mfma_f32_16x16x32_bf16(G3, f.B[0], acc[3], 0, 0, 0);
  acc[0] = __builtin_amdgcn_mfma_f32_16x16x32_bf16(Gm2, f.B[1], acc[0], 0, 0, 0);
  acc[1] = __builtin_amdgcn_mfma_f32_16x16x32_bf16(Gm1, f.B[1], acc[1], 0, 0, 0);
  acc[2] = __builtin_amdgcn_mfma_f32_16x16x32_bf16(G0, f.B[1], acc[2], 0, 0, 0);
  acc[3] = __builtin_amdgcn_mfma_f32_16x16x32_bf16(G1, f.B[1], acc[3], 0, 0, 0);
  Gm2 = G2; Gm1 = G3;
}
__device__ __forceinline__ void conv_part(const char* hb, const char* kvb, int d0, int d1, f32x4 (&acc)[4]) {
  bf16x8 Gm2, Gm1;
  if (d0 == 0) { Gm2 = (bf16x8){0, 0, 0, 0, 0, 0, 0, 0}; Gm1 = Gm2; }
  else {
    const char* p = hb + (4 * d0 - 2) * 32;
    Gm2 = cat8(*(const bf16x4*)p, *(const bf16x4*)(p + 8));
    Gm1 = cat8(*(const bf16x4*)(p + 32), *(const bf16x4*)(p + 40));
  }
  unsigned ha = lds_addr(hb) + d0 * 128, ka = lds_addr(kvb) - d0 * 16;
  ConvFr fa, fb;
  conv_load(fa, ha, ka);
#pragma unroll 1
  for (int dl = d0; dl < d1; dl += 2) {
    conv_load(fb, ha + 128, ka - 16);
    conv_wait<10>(fa);
    conv_mma(fa, Gm2, Gm1, acc);
    __builtin_amdgcn_sched_barrier(0);
    ha += 256; ka -= 32;
    conv_load(fa, ha, ka);
    conv_wait<10>(fb);
    conv_mma(fb, Gm2, Gm1, acc);
    __builtin_amdgcn_sched_barrier(0);
  }
  conv_wait<0>(fa);
}

__global__ __launch_bounds__(512) void conv_kernel(const u16* __restrict__ kvt, const u16* __restrict__ ssm_k,
                                                   const u16* __restrict__ ssm_D, u16* __restrict__ yout,
                                                   const u16* __restrict__ w1, const u16* __restrict__ w3,
                                                   const u16* __restrict__ w2) {
  if (blockIdx.x >= D_ / 4) {
    const int nb = gridDim.x - D_ / 4, b = blockIdx.x - D_ / 4;
    constexpr int NCH = D_ * F_ / 8;
    u32x4 a = (u32x4){0u, 0u, 0u, 0u};
    for (int i = b * 512 + threadIdx.x; i < NCH; i += nb * 512) {
      a ^= *(const u32x4*)(w1 + (size_t)i * 8);
      a ^= *(const u32x4*)(w3 + (size_t)i * 8);
      a ^= *(const u32x4*)(w2 + (size_t)i * 8);
    }
    if (a[0] == 0x9e3779b9u && a[1] == 0x7f4a7c15u && a[2] == 0x85ebca6bu && a[3] == 0xc2b2ae35u) yout[0] = 0;
    return;
  }

  __shared__ __attribute__((aligned(16))) char lds[4 * CV_KV_BYTES + 4 * CV_HC_BYTES + 16384];
  char* kvs = lds;
  char* hcs = lds + 4 * CV_KV_BYTES;
  char* red = lds + 4 * CV_KV_BYTES + 4 * CV_HC_BYTES;
  const int tid = threadIdx.x, lane = tid & 63, wave = tid >> 6;
  const int d0 = blockIdx.x * 4;
  KSTART(1);
  const int t0 = tid * 4;
#pragma unroll
  for (int ps = 0; ps < 2; ++ps) {
    int it = tid + 512 * ps, c = it >> 8, blk = (it >> 3) & 31, pl = it & 7;
    u32x4 w = *(const u32x4*)(kvt + ((size_t)(d0 + c) * 32 + blk) * 64 + pl * 8);
    *(u32x4*)(kvs + c * CV_KV_BYTES + (pl * CV_NB + blk + 16) * 16) = w;
  }
  for (int it = tid; it < 4 * 264; it += 512) {
    int c = it / 264, qp = it % 264;
    int q = qp - 4;
    const u16* hp = ssm_k + (size_t)(d0 + c) * S_;
    u32x4 a = (u32x4){0u, 0u, 0u, 0u}, b = (u32x4){0u, 0u, 0u, 0u};
    if (q >= 0 && q < 256) a = *(const u32x4*)(hp + q * 8);
    if (q + 1 >= 0 && q + 1 < 256) b = *(const u32x4*)(hp + (q + 1) * 8);
    char* dst = hcs + c * CV_HC_BYTES + qp * 16;
    *(u32x4*)(dst + 0 * CV_CS8 * 8) = a;
    u32x4 s1;
    s1[0] = __builtin_amdgcn_alignbit(a[1], a[0], 16);
    s1[1] = __builtin_amdgcn_alignbit(a[2], a[1], 16);
    s1[2] = __builtin_amdgcn_alignbit(a[3], a[2], 16);
    s1[3] = __builtin_amdgcn_alignbit(b[0], a[3], 16);
    *(u32x4*)(dst + 1 * CV_CS8 * 8) = s1;
    u32x4 s2 = (u32x4){a[1], a[2], a[3], b[0]};
    *(u32x4*)(dst + 2 * CV_CS8 * 8) = s2;
    u32x4 s3;
    s3[0] = s1[1]; s3[1] = s1[2]; s3[2] = s1[3];
    s3[3] = __builtin_amdgcn_alignbit(b[1], b[0], 16);
    *(u32x4*)(dst + 3 * CV_CS8 * 8) = s3;
  }
  {
    int c = tid >> 7, pl = (tid >> 4) & 7, b = tid & 15;
    *(u32x4*)(kvs + c * CV_KV_BYTES + (pl * CV_NB + b) * 16) = (u32x4){0u, 0u, 0u, 0u};
  }
  float dd[4];
#pragma unroll
  for (int c = 0; c < 4; ++c) dd[c] = bf2f(ssm_D[d0 + c]);
  __syncthreads();

  const int ch = wave & 3, part = wave >> 2;
  const int c16 = lane & 15, g = lane >> 4;
  f32x4 accA[4], accB[4];
#pragma unroll
  for (int i = 0; i < 4; ++i) { accA[i] = (f32x4){0, 0, 0, 0}; accB[i] = (f32x4){0, 0, 0, 0}; }
  {
    const char* kvb = kvs + ch * CV_KV_BYTES + (g * CV_NB + 16 + c16) * 16;
    const int r = (c16 + 1) & 3, w = (c16 + 1) >> 2;
    const char* hb = hcs + ch * CV_HC_BYTES + (r * CV_CS8 - 2 * g + 6 + w) * 8;
    if (part == 0) {
      conv_part(hb, kvb + 16 * 16, 0, 24, accA);
    } else {
      conv_part(hb, kvb, 0, 16, accA);
      conv_part(hb, kvb + 16 * 16, 24, 32, accB);
#pragma unroll
      for (int i = 0; i < 4; ++i) *(f32x4*)(red + ((ch * 4 + i) * 64 + lane) * 16) = accB[i];
    }
  }
  __syncthreads();
  {
    char* yt = hcs + ch * 4096;
    const int ct = part ? 0 : 1;
#pragma unroll
    for (int i = 0; i < 4; ++i) {
      f32x4 a = accA[i];
      if (part == 0) a += *(const f32x4*)(red + ((ch * 4 + i) * 64 + lane) * 16);
      int t = 64 * (16 * ct + c16) + 16 * i + 4 * g;
      u32x2 w;
      w[0] = pack2(a[0], a[1]);
      w[1] = pack2(a[2], a[3]);
      *(u32x2*)(yt + t * 2) = w;
    }
  }
  __syncthreads();
  {
    u32x2 yc[4];
    float kvf[4][4];
    const int plane = (t0 & 63) >> 3, blk = (t0 >> 6) + 16, hi = (t0 >> 2) & 1;
#pragma unroll
    for (int c = 0; c < 4; ++c) {
      yc[c] = *(const u32x2*)(hcs + c * 4096 + t0 * 2);
      u32x2 kw = *(const u32x2*)(kvs + c * CV_KV_BYTES + (plane * CV_NB + blk) * 16 + (hi ? 0 : 8));
      kvf[c][3] = lo16(kw[0]); kvf[c][2] = hi16(kw[0]); kvf[c][1] = lo16(kw[1]); kvf[c][0] = hi16(kw[1]);
    }
#pragma unroll
    for (int i = 0; i < 4; ++i) {
      float o[4];
#pragma unroll
      for (int c = 0; c < 4; ++c) {
        unsigned w = yc[c][i >> 1];
        float yv = bf2f((u16)((i & 1) ? (w >> 16) : (w & 0xffff)));
        o[c] = yv + rbf(kvf[c][i] * dd[c]);
      }
      u32x2 w;
      w[0] = pack2(o[0], o[1]);
      w[1] = pack2(o[2], o[3]);
      *(u32x2*)(yout + (size_t)(t0 + i) * D_ + d0) = w;
    }
  }
  KEND(1);
}

template <int CTRL> __device__ __forceinline__ float dpp_add(float v) {
  int o = __builtin_amdgcn_update_dpp(0, __float_as_int(v), CTRL, 0xf, 0xf, true);
  return v + __int_as_float(o);
}
__device__ __forceinline__ float sum64(float v) {
  v = dpp_add<0xB1>(v);
  v = dpp_add<0x4E>(v);
  v = dpp_add<0x141>(v);
  v = dpp_add<0x140>(v);
  int iv = __float_as_int(v);
  float a = __int_as_float(__builtin_amdgcn_readlane(iv, 0)), b = __int_as_float(__builtin_amdgcn_readlane(iv, 16));
  float c = __int_as_float(__builtin_amdgcn_readlane(iv, 32)), d = __int_as_float(__builtin_amdgcn_readlane(iv, 48));
  return (a + b) + (c + d);
}
__device__ __forceinline__ void layernorm9(const float (&x)[9], float (&o)[9]) {
  float s = 0.f;
#pragma unroll
  for (int i = 0; i < 9; ++i) s += x[i];
  s = sum64(s);
  const float mean = s * (1.0f / D_);
  float q = 0.f;
#pragma unroll
  for (int i = 0; i < 9; ++i) { float d = x[i] - mean; q += d * d; }
  q = sum64(q);
  const float var = q * (1.0f / D_);
  const float mb = rbf(mean);
  const float vb = rbf(var);
  const float sd = rbf(sqrtf(rbf(vb + 1e-5f)));
#pragma unroll
  for (int i = 0; i < 9; ++i) o[i] = rbf(rbf(x[i] - mb) / sd);
}
__device__ __forceinline__ void load9(const u16* __restrict__ p, int l, float (&x)[9]) {
  u32x2 a = *(const u32x2*)(p + 4 * l), b = *(const u32x2*)(p + 256 + 4 * l);
  u16 c = p[512 + l];
  x[0] = lo16(a[0]); x[1] = hi16(a[0]); x[2] = lo16(a[1]); x[3] = hi16(a[1]);
  x[4] = lo16(b[0]); x[5] = hi16(b[0]); x[6] = lo16(b[1]); x[7] = hi16(b[1]);
  x[8] = bf2f(c);
}

template <int MODE, int KID>
__global__ __launch_bounds__(256) void row_kernel(const u16* __restrict__ xin, const float* __restrict__ part,
                                                  const u16* __restrict__ y, u16* __restrict__ xout,
                                                  u16* __restrict__ xn_t) {
  KSTART(KID);
  const int l = threadIdx.x & 63;
  const int row = blockIdx.x * 4 + (threadIdx.x >> 6);
  float x[9], yv[9];
  load9(xin + (size_t)row * D_, l, x);
  if (MODE <= 1) load9(y + (size_t)row * D_, l, yv);
  if (MODE >= 1) {
    f32x4 a = (f32x4){0, 0, 0, 0}, b = (f32x4){0, 0, 0, 0};
    float c = 0.f;
#pragma unroll
    for (int s = 0; s < NSPLIT; ++s) {
      const float* pp = part + ((size_t)s * S_ + row) * D_;
      a += *(const f32x4*)(pp + 4 * l);
      b += *(const f32x4*)(pp + 256 + 4 * l);
      c += pp[512 + l];
    }
#pragma unroll
    for (int e = 0; e < 4; ++e) { x[e] = rbf(x[e] + rbf(a[e])); x[4 + e] = rbf(x[4 + e] + rbf(b[e])); }
    x[8] = rbf(x[8] + rbf(c));
  }
  if (MODE <= 1) {
    float n[9];
    layernorm9(x, n);
#pragma unroll
    for (int i = 0; i < 9; ++i) x[i] = rbf(x[i] + rbf(n[i] * yv[i]));
  }
  {
    u16* po = xout + (size_t)row * D_;
    u32x2 w;
    w[0] = cvt2(x[0], x[1]); w[1] = cvt2(x[2], x[3]);
    *(u32x2*)(po + 4 * l) = w;
    w[0] = cvt2(x[4], x[5]); w[1] = cvt2(x[6], x[7]);
    *(u32x2*)(po + 256 + 4 * l) = w;
    po[512 + l] = f2bf(x[8]);
  }
  if (MODE <= 1) {
    float n[9];
    layernorm9(x, n);
    const int tt = row >> 4, c = row & 15;
    char* base = (char*)xn_t + (size_t)tt * KT1 * 1024 + c * 16;
    u32x2 w;
    w[0] = cvt2(n[0], n[1]); w[1] = cvt2(n[2], n[3]);
    *(u32x2*)(base + (l >> 3) * 1024 + (l & 3) * 256 + ((l >> 2) & 1) * 8) = w;
    w[0] = cvt2(n[4], n[5]); w[1] = cvt2(n[6], n[7]);
    *(u32x2*)(base + (8 + (l >> 3)) * 1024 + (l & 3) * 256 + ((l >> 2) & 1) * 8) = w;
    *(u16*)(base + (16 + (l >> 5)) * 1024 + ((l >> 2) & 3) * 256 + ((l >> 4) & 1) * 8 + (l & 3) * 2) = f2bf(n[8]);
  }
  KEND(KID);
}

static constexpr int STG = 34816;
static constexpr int NSTG = 4;

__device__ __forceinline__ u32x2 silu_mul4(f32x4 g, f32x4 u) {
  u32x2 o;
#pragma unroll
  for (int i = 0; i < 2; ++i) {
    unsigned gw = cvt2(g[2 * i], g[2 * i + 1]);
    unsigned uw = cvt2(u[2 * i], u[2 * i + 1]);
    float g0 = lo16(gw), g1 = hi16(gw);
    float s0 = g0 * __builtin_amdgcn_rcpf(1.0f + __builtin_amdgcn_exp2f(g0 * -1.44269504089f));
    float s1 = g1 * __builtin_amdgcn_rcpf(1.0f + __builtin_amdgcn_exp2f(g1 * -1.44269504089f));
    unsigned sw = cvt2(s0, s1);
    o[i] = cvt2(lo16(sw) * lo16(uw), hi16(sw) * hi16(uw));
  }
  return o;
}

template <int N> __device__ __forceinline__ void wait_vm2(bf16x8& a, bf16x8& b) {
  asm volatile("s_waitcnt vmcnt(%2)" : "+v"(a), "+v"(b) : "i"(N) : "memory");
}
__device__ __forceinline__ void wait_vm2_dyn(int n, bf16x8& a, bf16x8& b) {
  switch (n) {
    case 0: wait_vm2<0>(a, b); break;
    case 1: wait_vm2<1>(a, b); break;
    case 2: wait_vm2<2>(a, b); break;
    case 3: wait_vm2<3>(a, b); break;
    case 4: wait_vm2<4>(a, b); break;
    case 5: wait_vm2<5>(a, b); break;
    case 6: wait_vm2<6>(a, b); break;
    case 7: wait_vm2<7>(a, b); break;
    case 8: wait_vm2<8>(a, b); break;
    case 9: wait_vm2<9>(a, b); break;
    case 10: wait_vm2<10>(a, b); break;
    case 11: wait_vm2<11>(a, b); break;
    case 12: wait_vm2<12>(a, b); break;
    case 13: wait_vm2<13>(a, b); break;
    case 14: wait_vm2<14>(a, b); break;
    case 15: wait_vm2<15>(a, b); break;
    case 16: wait_vm2<16>(a, b); break;
    case 17: wait_vm2<17>(a, b); break;
    case 18: wait_vm2<18>(a, b); break;
    case 19: wait_vm2<19>(a, b); break;
    case 20: wait_vm2<20>(a, b); break;
    case 21: wait_vm2<21>(a, b); break;
    case 22: wait_vm2<22>(a, b); break;
    case 23: wait_vm2<23>(a, b); break;
    case 24: wait_vm2<24>(a, b); break;
    case 25: wait_vm2<25>(a, b); break;
    case 26: wait_vm2<26>(a, b); break;
    case 27: wait_vm2<27>(a, b); break;
    case 28: wait_vm2<28>(a, b); break;
    case 29: wait_vm2<29>(a, b); break;
    case 30: wait_vm2<30>(a, b); break;
    case 31: wait_vm2<31>(a, b); break;
    default: wait_vm2<0>(a, b); break;
  }
}
template <int N> __device__ __forceinline__ void wait_vm() {
  asm volatile("s_waitcnt vmcnt(%0)" :: "i"(N) : "memory");
}
__device__ __forceinline__ bf16x8 gload16(const void* p) {
  bf16x8 r;
  asm volatile("global_load_dwordx4 %0, %1, off" : "=v"(r) : "v"(p) : "memory");
  return r;
}
__device__ __forceinline__ u32x2 swiglu4(f32x4 g, f32x4 u) {
  f32x4 e;
#pragma unroll
  for (int i = 0; i < 4; ++i) e[i] = __builtin_amdgcn_exp2f(g[i] * -1.44269504089f);
  e = e + 1.0f;
#pragma unroll
  for (int i = 0; i < 4; ++i) e[i] = __builtin_amdgcn_rcpf(e[i]);
  f32x4 h = (g * u) * e;
  u32x2 o;
  o[0] = cvt2(h[0], h[1]);
  o[1] = cvt2(h[2], h[3]);
  return o;
}

template <int KID>
__global__ __launch_bounds__(512, 2) void gemm1_kernel(const u16* __restrict__ xn_t, const u16* __restrict__ w1,
                                                       const u16* __restrict__ w3, u16* __restrict__ h_t) {
  __shared__ __attribute__((aligned(16))) char lds[NSTG * STG];
  const int tid = threadIdx.x, lane = tid & 63;
  const int wave = __builtin_amdgcn_readfirstlane(tid >> 6);
  const int wm = wave & 3, wn = wave >> 2;
  const int bid = blockIdx.x, xcd = bid & 7, idx = bid >> 3;
  const int ft = xcd * 4 + (idx & 3), mt = idx >> 2;
  KSTART(KID);
  const char* xs = (const char*)xn_t + ((size_t)(mt * 16 + 2 * wave) * KT1) * 1024 + lane * 16;
  const char *ws0, *ws1, *ws2;
  {
    const int colA = 32 * (9 * (ft >> 1) + 4 * (ft & 1)), colB = 32 * (9 * (ft >> 1) + 8) + 16 * (ft & 1) - 128;
    int ci = wave * 128 + lane;
    int img = ci / 576, rem = ci % 576, row = rem / 18, cc = rem % 18;
    ws0 = (const char*)((img ? w3 : w1) + (size_t)row * F_ + (cc < 16 ? colA : colB) + cc * 8);
    ci += 64;
    img = ci / 576; rem = ci % 576; row = rem / 18; cc = rem % 18;
    ws1 = (const char*)((img ? w3 : w1) + (size_t)row * F_ + (cc < 16 ? colA : colB) + cc * 8);
    ci = 1024 + wave * 16 + (lane & 15);
    img = ci / 576; rem = ci % 576; row = rem / 18; cc = rem % 18;
    ws2 = (const char*)((img ? w3 : w1) + (size_t)row * F_ + (cc < 16 ? colA : colB) + cc * 8);
  }
  constexpr size_t WSTEP = (size_t)32 * F_ * 2;
  const unsigned lbase = lds_addr(lds);
  auto issue = [&](int s) {
    unsigned sb = lbase + (s & 3) * STG;
    glds16(xs + s * 1024, sb + (2 * wave) * 1024);
    glds16(xs + KT1 * 1024 + s * 1024, sb + (2 * wave + 1) * 1024);
    glds16(ws0 + s * WSTEP, sb + 16384 + wave * 2048);
    glds16(ws1 + s * WSTEP, sb + 16384 + wave * 2048 + 1024);
    if (lane < 16) glds16(ws2 + s * WSTEP, sb + 16384 + 16384 + wave * 256);
  };
  auto issueP = [&](int s, int part_) {
    unsigned sb = lbase + (s & 3) * STG;
    if (part_ == 0) glds16(xs + s * 1024, sb + (2 * wave) * 1024);
    if (part_ == 1) glds16(xs + KT1 * 1024 + s * 1024, sb + (2 * wave + 1) * 1024);
    if (part_ == 2) glds16(ws0 + s * WSTEP, sb + 16384 + wave * 2048);
    if (part_ == 3) glds16(ws1 + s * WSTEP, sb + 16384 + wave * 2048 + 1024);
    if (part_ == 4) { if (lane < 16) glds16(ws2 + s * WSTEP, sb + 16384 + 16384 + wave * 256); }
  };

  f32x4 acc[9][4];
#pragma unroll
  for (int n = 0; n < 9; ++n)
#pragma unroll
    for (int m = 0; m < 4; ++m) acc[n][m] = (f32x4){0, 0, 0, 0};

  const int g = lane >> 4, q = (lane & 15) >> 2, p = lane & 3;
  const int cb = wn * 64, cm = 128 + 8 * wn;
  const int xoff = wm * 4096 + lane * 16;
  const int woff = 16384 + (4 * g + q) * 288 + (cb + 4 * p) * 2;
  const int moff = 16384 + (p >> 1) * 9216 + (4 * g + q) * 288 + (cm + 4 * (p & 1)) * 2;

  issue(0); issue(1); issue(2);
  auto wread = [&](const char* sb, int n) -> bf16x8 {
    const char* wp = (n < 8) ? (sb + woff + (n >> 2) * 9216 + (n & 3) * 32) : (sb + moff);
    return cat8(tr_read(wp), tr_read(wp + 16 * 288));
  };
  bf16x8 xa[4], xb[4], fa, fb, fc;
  WAIT_V(10);
  __builtin_amdgcn_s_barrier();
  asm volatile("" ::: "memory");
#pragma unroll
  for (int m = 0; m < 4; ++m) xa[m] = *(const bf16x8*)(lds + xoff + m * 1024);
  fa = wread(lds, 0);
  fb = wread(lds, 1);

#define SB0 __builtin_amdgcn_sched_barrier(0)
#define MM(n, w, x) _Pragma("unroll") for (int m = 0; m < 4; ++m) acc[n][m] = __builtin_amdgcn_mfma_f32_16x16x32_bf16(w, x[m], acc[n][m], 0, 0, 0)
  auto body = [&](int s, bf16x8 (&xc)[4], bf16x8 (&xn)[4], bool last) {
    const char* sb = lds + (s & 3) * STG;
    const char* sn = lds + ((s + 1) & 3) * STG;
    fc = wread(sb, 2); SB0;
    MM(0, fa, xc); SB0; fa = wread(sb, 3); SB0;
    if (s >= 1 && s + 2 < KT1) issueP(s + 2, 3);
    SB0;
    MM(1, fb, xc); SB0; fb = wread(sb, 4); SB0;
    if (s >= 1 && s + 2 < KT1) issueP(s + 2, 4);
    SB0;
    MM(2, fc, xc); SB0; fc = wread(sb, 5); SB0;
    MM(3, fa, xc); SB0; fa = wread(sb, 6); SB0;
    MM(4, fb, xc); SB0; fb = wread(sb, 7); SB0;
    MM(5, fc, xc); SB0; fc = wread(sb, 8); SB0;
    if (!last) {
      if (s < KT1 - 2) WAIT_V(5); else WAIT_V(0);
      __builtin_amdgcn_s_barrier();
      asm volatile("" ::: "memory");
#pragma unroll
      for (int m = 0; m < 4; ++m) xn[m] = *(const bf16x8*)(sn + xoff + m * 1024);
      SB0;
    }
    if (s + 3 < KT1) issueP(s + 3, 0);
    SB0;
    MM(6, fa, xc); SB0; if (!last) fa = wread(sn, 0); SB0;
    if (s + 3 < KT1) issueP(s + 3, 1);
    SB0;
    MM(7, fb, xc); SB0; if (!last) fb = wread(sn, 1); SB0;
    if (s + 3 < KT1) issueP(s + 3, 2);
    SB0;
    MM(8, fc, xc); SB0;
  };
  for (int s = 0; s < KT1 - 2; s += 2) {
    body(s, xa, xb, false);
    body(s + 1, xb, xa, false);
  }
  body(KT1 - 2, xa, xb, false);
  body(KT1 - 1, xb, xa, true);
#undef MM

  const int c = lane & 15;
  const int P0 = 9 * (ft >> 1) + 4 * (ft & 1) + 2 * wn;
  const int PX = 9 * (ft >> 1) + 8;
#pragma unroll
  for (int m = 0; m < 4; ++m) {
    const int tt = mt * 16 + wm * 4 + m;
    char* hb = (char*)h_t + (size_t)tt * KT2 * 1024;
#pragma unroll
    for (int n = 0; n < 2; ++n) {
      u32x2 a = swiglu4(acc[2 * n][m], acc[4 + 2 * n][m]);
      u32x2 b = swiglu4(acc[2 * n + 1][m], acc[5 + 2 * n][m]);
      st16u(hb + (size_t)(P0 + n) * 1024 + lane * 16, (u32x4){a[0], a[1], b[0], b[1]});
    }
    {
      f32x4 o;
#pragma unroll
      for (int r = 0; r < 4; ++r) o[r] = __shfl_xor(acc[8][m][r], 32, 64);
      if (lane < 32) {
        u32x2 w = swiglu4(acc[8][m], o);
        int gs = 2 * wn + g;
        *(u32x2*)(hb + (size_t)PX * 1024 + (gs * 16 + c) * 16 + (ft & 1) * 8) = w;
      }
    }
  }
  KEND(KID);
}

static constexpr int WST = 18432;
template <int KID>
__global__ __launch_bounds__(512, 2) void gemm2_kernel(const u16* __restrict__ h_t, const u16* __restrict__ w2,
                                                       float* __restrict__ part) {
  __shared__ __attribute__((aligned(16))) char lds[8 * WST];
  const int tid = threadIdx.x, lane = tid & 63;
  const int wave = __builtin_amdgcn_readfirstlane(tid >> 6);
  const int wm = wave & 3, wk = wave >> 2;
  const int bid = blockIdx.x, xcd = bid & 7, idx = bid >> 3;
  const int ks = xcd >> 1;
  const int nt = idx & 3, mt = (xcd & 1) * 8 + (idx >> 2);
  const int kt0 = ks * 36;
  KSTART(KID);

  const char* xs0 = (const char*)h_t + ((size_t)(mt * 8 + wm * 2) * KT2 + kt0 + wk * 18) * 1024 + lane * 16;
  const char* xs1 = xs0 + (size_t)KT2 * 1024;
  const char *ws0, *ws1, *ws2;
  {
    int ci = wave * 128 + lane;
    int img = ci / 576, rem = ci % 576, row = rem / 18, cc = rem % 18;
    ws0 = (const char*)(w2 + (size_t)((kt0 + img * 18) * 32 + row) * D_ + nt * 144 + cc * 8);
    ci += 64;
    img = ci / 576; rem = ci % 576; row = rem / 18; cc = rem % 18;
    ws1 = (const char*)(w2 + (size_t)((kt0 + img * 18) * 32 + row) * D_ + nt * 144 + cc * 8);
    ci = 1024 + wave * 16 + (lane & 15);
    img = ci / 576; rem = ci % 576; row = rem / 18; cc = rem % 18;
    ws2 = (const char*)(w2 + (size_t)((kt0 + img * 18) * 32 + row) * D_ + nt * 144 + cc * 8);
  }
  constexpr size_t WSTEP = (size_t)32 * D_ * 2;
  const unsigned lbase = lds_addr(lds);
  auto issueW = [&](int s) {
    unsigned sb = lbase + (s & 7) * WST;
    glds16(ws0, sb + wave * 2048);
    glds16(ws1, sb + wave * 2048 + 1024);
    if (lane < 16) glds16(ws2, sb + 16384 + wave * 256);
    ws0 += WSTEP; ws1 += WSTEP; ws2 += WSTEP;
    asm volatile("" : "+v"(ws0), "+v"(ws1), "+v"(ws2));
  };
  auto loadX = [&](bf16x8 (&x)[2]) {
    x[0] = gload16(xs0);
    x[1] = gload16(xs1);
    xs0 += 1024; xs1 += 1024;
    asm volatile("" : "+v"(xs0), "+v"(xs1));
  };

  f32x4 acc[9][2];
#pragma unroll
  for (int n = 0; n < 9; ++n) { acc[n][0] = (f32x4){0, 0, 0, 0}; acc[n][1] = (f32x4){0, 0, 0, 0}; }

  const int g = lane >> 4, q = (lane & 15) >> 2, p = lane & 3;
  const int woff = wk * 9216 + (4 * g + q) * 288 + (4 * p) * 2;

  bf16x8 xr[6][2];
#pragma unroll
  for (int j = 0; j < 5; ++j) { loadX(xr[j]); issueW(j); }
  issueW(5); issueW(6);
  wait_vm2<3 + 4 * 5 + 6>(xr[0][0], xr[0][1]);
  __builtin_amdgcn_s_barrier();
  asm volatile("" ::: "memory");

  auto wread = [&](const char* sb, int n) -> bf16x8 {
    const char* wp = sb + n * 32;
    return cat8(tr_read(wp), tr_read(wp + 16 * 288));
  };
#define SB0 __builtin_amdgcn_sched_barrier(0)
#define MM(n, w, x) do { acc[n][0] = __builtin_amdgcn_mfma_f32_16x16x32_bf16(w, x[0], acc[n][0], 0, 0, 0); \
                         acc[n][1] = __builtin_amdgcn_mfma_f32_16x16x32_bf16(w, x[1], acc[n][1], 0, 0, 0); } while (0)
  bf16x8 fa = wread(lds + woff, 0), fb = wread(lds + woff, 1), fc;
  auto stage = [&](int s, bf16x8 (&xc)[2], bf16x8 (&xn)[2], bf16x8 (&xp)[2], bool last) {
    const char* sb = lds + (s & 7) * WST + woff;
    const char* sn = lds + ((s + 1) & 7) * WST + woff;
    fc = wread(sb, 2); SB0;
    MM(0, fa, xc); fa = wread(sb, 3); SB0;
    MM(1, fb, xc); fb = wread(sb, 4); SB0;
    MM(2, fc, xc); fc = wread(sb, 5); SB0;
    MM(3, fa, xc); fa = wread(sb, 6); SB0;
    MM(4, fb, xc); fb = wread(sb, 7); SB0;
    MM(5, fc, xc); fc = wread(sb, 8); SB0;
    if (!last) {
      if (s <= 11) wait_vm<18>();
      else if (s == 12) wait_vm<15>();
      else if (s == 13) wait_vm<12>();
      else if (s == 14) wait_vm<7>();
      else if (s == 15) wait_vm<2>();
      else wait_vm<0>();
      asm volatile("" : "+v"(xn[0]), "+v"(xn[1]));
      __builtin_amdgcn_s_barrier();
      asm volatile("" ::: "memory");
      SB0;
    }
    MM(6, fa, xc); if (!last) fa = wread(sn, 0); SB0;
    if (s + 5 < 18) loadX(xp);
    if (s + 7 < 18) issueW(s + 7);
    SB0;
    MM(7, fb, xc); if (!last) fb = wread(sn, 1); SB0;
    MM(8, fc, xc); SB0;
  };
#pragma unroll 1
  for (int s = 0; s < 18; s += 6) {
    stage(s + 0, xr[0], xr[1], xr[5], false);
    stage(s + 1, xr[1], xr[2], xr[0], false);
    stage(s + 2, xr[2], xr[3], xr[1], false);
    stage(s + 3, xr[3], xr[4], xr[2], false);
    stage(s + 4, xr[4], xr[5], xr[3], false);
    stage(s + 5, xr[5], xr[0], xr[4], s + 5 == 17);
  }
#undef MM
  BAR();
  f32x4* red = (f32x4*)lds;
  int tz = threadIdx.x; asm volatile("" : "+v"(tz));
  const int ln = tz & 63;
#pragma unroll
  for (int n = 0; n < 9; ++n) {
    if ((n < 5) == (wk == 1)) {
#pragma unroll
      for (int m = 0; m < 2; ++m) red[(wm * 18 + n * 2 + m) * 64 + ln] = acc[n][m];
    }
  }
  BAR();
  {
    const int c = ln & 15;
#pragma unroll
    for (int m = 0; m < 2; ++m) {
      const int token = mt * 128 + wm * 32 + m * 16 + c;
      float* dst = part + ((size_t)ks * S_ + token) * D_ + nt * 144 + 4 * (ln >> 4);
#pragma unroll
      for (int n = 0; n < 9; ++n) {
        if ((n < 5) == (wk == 0)) {
          f32x4 o = acc[n][m] + red[(wm * 18 + n * 2 + m) * 64 + ln];
          st16(dst + n * 16, o);
        }
      }
    }
  }
  KEND(KID);
}

extern "C" void kernel_launch(void* const* d_in, const int* in_sizes, int n_in,
                              void* d_out, int out_size, void* d_ws, size_t ws_size,
                              hipStream_t stream) {
  const u16* A_q = (const u16*)d_in[0];
  const u16* A_k = (const u16*)d_in[1];
  const u16* A_v = (const u16*)d_in[2];
  const u16* A_shift = (const u16*)d_in[3];
  const u16* A_ssmk = (const u16*)d_in[4];
  const u16* A_ssmD = (const u16*)d_in[5];
  const u16* B_w1 = (const u16*)d_in[7];
  const u16* B_w3 = (const u16*)d_in[8];
  const u16* B_w2 = (const u16*)d_in[9];
  u16* out = (u16*)d_out;

  char* ws = (char*)d_ws;
  constexpr size_t XB = (size_t)S_ * D_ * 2;
  u16* y = (u16*)(ws);
  u16* xa = (u16*)(ws + XB);
  u16* xb = (u16*)(ws + 2 * XB);
  u16* xn = (u16*)(ws + 3 * XB);
  u16* h = (u16*)(ws + 4 * XB);
  float* part = (float*)(ws + 4 * XB + (size_t)S_ * F_ * 2);

  kvt_kernel<<<288, 256, 0, stream>>>(A_k, A_v, A_shift, xb);
  conv_kernel<<<256, 512, 0, stream>>>(xb, A_ssmk, A_ssmD, y, B_w1, B_w3, B_w2);
  row_kernel<0, KIDV(2)><<<S_ / 4, 256, 0, stream>>>(A_q, nullptr, y, xa, xn);
  gemm1_kernel<KIDV(3)><<<256, 512, 0, stream>>>(xn, B_w1, B_w3, h);
  gemm2_kernel<KIDV(4)><<<256, 512, 0, stream>>>(h, B_w2, part);
  row_kernel<1, KIDV(5)><<<S_ / 4, 256, 0, stream>>>(xa, part, y, xb, xn);
  gemm1_kernel<KIDV(6)><<<256, 512, 0, stream>>>(xn, B_w1, B_w3, h);
  gemm2_kernel<KIDV(7)><<<256, 512, 0, stream>>>(h, B_w2, part);
  row_kernel<2, KIDV(8)><<<S_ / 4, 256, 0, stream>>>(xb, part, nullptr, out, nullptr);
}
```

```cpp
#include <hip/hip_runtime.h>
#include <hip/hip_bf16.h>
#include <stdint.h>
#define ST_SC1 1
#define G1MAP 0

typedef __attribute__((ext_vector_type(8))) short bf16x8;
typedef __attribute__((ext_vector_type(4))) short bf16x4;
typedef __attribute__((ext_vector_type(4))) float f32x4;
typedef __attribute__((ext_vector_type(4))) unsigned u32x4;
typedef __attribute__((ext_vector_type(2))) unsigned u32x2;
typedef unsigned short u16;

static constexpr int S_ = 2048, D_ = 576, F_ = 4608;
static constexpr int KT1 = D_ / 32;
static constexpr int KT2 = F_ / 32;
static constexpr int NSPLIT = 4;

#define LDS_AS __attribute__((address_space(3)))
#define GLB_AS __attribute__((address_space(1)))

__device__ __forceinline__ float bf2f(u16 v) { return __uint_as_float(((unsigned)v) << 16); }
__device__ __forceinline__ u16 f2bf(float f) {
  __hip_bfloat16 b = __float2bfloat16(f);
  return *reinterpret_cast<u16*>(&b);
}
__device__ __forceinline__ float rbf(float f) { return bf2f(f2bf(f)); }
__device__ __forceinline__ unsigned pack2(float lo, float hi) {
  return (unsigned)f2bf(lo) | ((unsigned)f2bf(hi) << 16);
}

typedef __attribute__((ext_vector_type(2))) float f32x2;
typedef __attribute__((ext_vector_type(2))) __bf16 xbf16x2;
__device__ __forceinline__ unsigned cvt2(float lo, float hi) {
  f32x2 v = {lo, hi};
  xbf16x2 b = __builtin_convertvector(v, xbf16x2);
  return __builtin_bit_cast(unsigned, b);
}
__device__ __forceinline__ float lo16(unsigned w) { return __uint_as_float(w << 16); }
__device__ __forceinline__ float hi16(unsigned w) { return __uint_as_float(w & 0xffff0000u); }
__device__ __forceinline__ unsigned lds_addr(const void* l) { return (unsigned)(size_t)(LDS_AS const char*)l; }
__device__ __forceinline__ void st8(void* p, u32x2 v) {
  asm volatile("global_store_dwordx2 %0, %1, off sc0 sc1" :: "v"(p), "v"(v) : "memory");
}
__device__ __forceinline__ void st16u(void* p, u32x4 v) {
  asm volatile("global_store_dwordx4 %0, %1, off sc0 sc1\n\ts_nop 1" :: "v"(p), "v"(v) : "memory");
}
__device__ __forceinline__ void st16(void* p, f32x4 v) {
  asm volatile("global_store_dwordx4 %0, %1, off sc0 sc1\n\ts_nop 1" :: "v"(p), "v"(v) : "memory");
}
__device__ __forceinline__ void glds16(const void* g, unsigned l) {
  unsigned keep;
  l = __builtin_amdgcn_readfirstlane(l);
  asm volatile("s_mov_b32 %0, m0\n\ts_mov_b32 m0, %2\n\ts_nop 0\n\tglobal_load_lds_dwordx4 %1, off\n\ts_mov_b32 m0, %0"
               : "=&s"(keep) : "v"(g), "s"(l) : "memory");
}
__device__ __forceinline__ bf16x4 tr_read(const char* p) {
  return __builtin_amdgcn_ds_read_tr16_b64_v4i16((LDS_AS bf16x4*)p);
}
__device__ __forceinline__ bf16x8 cat8(bf16x4 a, bf16x4 b) {
  return __builtin_shufflevector(a, b, 0, 1, 2, 3, 4, 5, 6, 7);
}
#define KSTART(id)
#define KEND(id)
#define KIDV(x) 0
#define WAIT_V(n) asm volatile("s_waitcnt vmcnt(" #n ")" ::: "memory")
#define BAR() do { asm volatile("s_waitcnt lgkmcnt(0)" ::: "memory"); __builtin_amdgcn_s_barrier(); asm volatile("" ::: "memory"); } while (0)

static constexpr int CV_NB = 48;
static constexpr int CV_KV_BYTES = 8 * CV_NB * 16;
static constexpr int CV_CS8 = 552;
static constexpr int CV_HC_BYTES = 4 * CV_CS8 * 8;

__global__ __launch_bounds__(256) void kvt_kernel(const u16* __restrict__ kin, const u16* __restrict__ vin,
                                                  const u16* __restrict__ shift_k, u16* __restrict__ kvt) {
  __shared__ __attribute__((aligned(16))) u16 tl[64][72];
  const int tid = threadIdx.x;
  KSTART(0);
  const int blk = blockIdx.x & 31, cht = blockIdx.x >> 5;
  const int cg = tid & 7, c0 = cht * 64 + cg * 8;
  float sk[8][4];
#pragma unroll
  for (int e = 0; e < 8; ++e) {
    u32x2 w = *(const u32x2*)(shift_k + (c0 + e) * 4);
    sk[e][0] = lo16(w[0]); sk[e][1] = hi16(w[0]); sk[e][2] = lo16(w[1]); sk[e][3] = hi16(w[1]);
  }
#pragma unroll
  for (int ps = 0; ps < 2; ++ps) {
    const int r = (tid >> 3) + 32 * ps, t = blk * 64 + r;
    u32x4 kr[4];
#pragma unroll
    for (int j = 0; j < 4; ++j) {
      int tt = t - 3 + j;
      if (tt >= 0) kr[j] = *(const u32x4*)(kin + (size_t)tt * D_ + c0);
      else kr[j] = (u32x4){0u, 0u, 0u, 0u};
    }
    u32x4 vr = *(const u32x4*)(vin + (size_t)t * D_ + c0);
    const int slot = (r & ~7) + 7 - (r & 7);
#pragma unroll
    for (int e = 0; e < 8; ++e) {
      float acc = 0.f;
#pragma unroll
      for (int j = 0; j < 4; ++j) {
        float kvv = (e & 1) ? hi16(kr[j][e >> 1]) : lo16(kr[j][e >> 1]);
        acc = rbf(acc + rbf(kvv * sk[e][3 - j]));
      }
      float vv = (e & 1) ? hi16(vr[e >> 1]) : lo16(vr[e >> 1]);
      tl[cg * 8 + e][slot] = f2bf(acc * vv);
    }
  }
  __syncthreads();
#pragma unroll
  for (int ps = 0; ps < 2; ++ps) {
    const int ch = (tid >> 3) + 32 * ps, pl = tid & 7;
    u32x4 w = *(const u32x4*)(&tl[ch][pl * 8]);
    *(u32x4*)(kvt + ((size_t)(cht * 64 + ch) * 32 + blk) * 64 + pl * 8) = w;
  }
  KEND(0);
}

struct ConvFr { bf16x4 G[8]; bf16x8 B[2]; };
__device__ __forceinline__ void conv_load(ConvFr& f, unsigned ha, unsigned ka) {
  asm volatile("ds_read_b64 %0, %10\n\tds_read_b64 %1, %10 offset:8\n\t"
               "ds_read_b64 %2, %10 offset:32\n\tds_read_b64 %3, %10 offset:40\n\t"
               "ds_read_b64 %4, %10 offset:64\n\tds_read_b64 %5, %10 offset:72\n\t"
               "ds_read_b64 %6, %10 offset:96\n\tds_read_b64 %7, %10 offset:104\n\t"
               "ds_read_b128 %8, %11\n\tds_read_b128 %9, %11 offset:3072"
               : "=&v"(f.G[0]), "=&v"(f.G[1]), "=&v"(f.G[2]), "=&v"(f.G[3]), "=&v"(f.G[4]), "=&v"(f.G[5]), "=&v"(f.G[6]), "=&v"(f.G[7]),
                 "=&v"(f.B[0]), "=&v"(f.B[1])
               : "v"(ha), "v"(ka) : "memory");
}
template <int N> __device__ __forceinline__ void conv_wait(ConvFr& f) {
  asm volatile("s_waitcnt lgkmcnt(%10)"
               : "+v"(f.G[0]), "+v"(f.G[1]), "+v"(f.G[2]), "+v"(f.G[3]), "+v"(f.G[4]), "+v"(f.G[5]), "+v"(f.G[6]), "+v"(f.G[7]),
                 "+v"(f.B[0]), "+v"(f.B[1]) : "i"(N) : "memory");
}
__device__ __forceinline__ void conv_mma(const ConvFr& f, bf16x8& Gm2, bf16x8& Gm1, f32x4 (&acc)[4]) {
  bf16x8 G0 = cat8(f.G[0], f.G[1]), G1 = cat8(f.G[2], f.G[3]), G2 = cat8(f.G[4], f.G[5]), G3 = cat8(f.G[6], f.G[7]);
  acc[0] = __builtin_amdgcn_mfma_f32_16x16x32_bf16(G0, f.B[0], acc[0], 0, 0, 0);
  acc[1] = __builtin_amdgcn_mfma_f32_16x16x32_bf16(G1, f.B[0], acc[1], 0, 0, 0);
  acc[2] = __builtin_amdgcn_mfma_f32_16x16x32_bf16(G2, f.B[0], acc[2], 0, 0, 0);
  acc[3] = __builtin_amdgcn_mfma_f32_16x16x32_bf16(G3, f.B[0], acc[3], 0, 0, 0);
  acc[0] = __builtin_amdgcn_mfma_f32_16x16x32_bf16(Gm2, f.B[1], acc[0], 0, 0, 0);
  acc[1] = __builtin_amdgcn_mfma_f32_16x16x32_bf16(Gm1, f.B[1], acc[1], 0, 0, 0);
  acc[2] = __builtin_amdgcn_mfma_f32_16x16x32_bf16(G0, f.B[1], acc[2], 0, 0, 0);
  acc[3] = __builtin_amdgcn_mfma_f32_16x16x32_bf16(G1, f.B[1], acc[3], 0, 0, 0);
  Gm2 = G2; Gm1 = G3;
}
__device__ __forceinline__ void conv_part(const char* hb, const char* kvb, int d0, int d1, f32x4 (&acc)[4]) {
  bf16x8 Gm2, Gm1;
  if (d0 == 0) { Gm2 = (bf16x8){0, 0, 0, 0, 0, 0, 0, 0}; Gm1 = Gm2; }
  else {
    const char* p = hb + (4 * d0 - 2) * 32;
    Gm2 = cat8(*(const bf16x4*)p, *(const bf16x4*)(p + 8));
    Gm1 = cat8(*(const bf16x4*)(p + 32), *(const bf16x4*)(p + 40));
  }
  unsigned ha = lds_addr(hb) + d0 * 128, ka = lds_addr(kvb) - d0 * 16;
  ConvFr fa, fb;
  conv_load(fa, ha, ka);
#pragma unroll 1
  for (int dl = d0; dl < d1; dl += 2) {
    conv_load(fb, ha + 128, ka - 16);
    conv_wait<10>(fa);
    conv_mma(fa, Gm2, Gm1, acc);
    __builtin_amdgcn_sched_barrier(0);
    ha += 256; ka -= 32;
    conv_load(fa, ha, ka);
    conv_wait<10>(fb);
    conv_mma(fb, Gm2, Gm1, acc);
    __builtin_amdgcn_sched_barrier(0);
  }
  conv_wait<0>(fa);
}

__global__ __launch_bounds__(512) void conv_kernel(const u16* __restrict__ kvt, const u16* __restrict__ ssm_k,
                                                   const u16* __restrict__ ssm_D, u16* __restrict__ yout,
                                                   const u16* __restrict__ w1, const u16* __restrict__ w3,
                                                   const u16* __restrict__ w2) {
  if (blockIdx.x >= D_ / 4) {
    const int nb = gridDim.x - D_ / 4, b = blockIdx.x - D_ / 4;
    constexpr int NCH = D_ * F_ / 8;
    u32x4 a = (u32x4){0u, 0u, 0u, 0u};
    for (int i = b * 512 + threadIdx.x; i < NCH; i += nb * 512) {
      a ^= *(const u32x4*)(w1 + (size_t)i * 8);
      a ^= *(const u32x4*)(w3 + (size_t)i * 8);
      a ^= *(const u32x4*)(w2 + (size_t)i * 8);
    }
    if (a[0] == 0x9e3779b9u && a[1] == 0x7f4a7c15u && a[2] == 0x85ebca6bu && a[3] == 0xc2b2ae35u) yout[0] = 0;
    return;
  }

  __shared__ __attribute__((aligned(16))) char lds[4 * CV_KV_BYTES + 4 * CV_HC_BYTES + 16384];
  char* kvs = lds;
  char* hcs = lds + 4 * CV_KV_BYTES;
  char* red = lds + 4 * CV_KV_BYTES + 4 * CV_HC_BYTES;
  const int tid = threadIdx.x, lane = tid & 63, wave = tid >> 6;
  const int d0 = blockIdx.x * 4;
  KSTART(1);
  const int t0 = tid * 4;
#pragma unroll
  for (int ps = 0; ps < 2; ++ps) {
    int it = tid + 512 * ps, c = it >> 8, blk = (it >> 3) & 31, pl = it & 7;
    u32x4 w = *(const u32x4*)(kvt + ((size_t)(d0 + c) * 32 + blk) * 64 + pl * 8);
    *(u32x4*)(kvs + c * CV_KV_BYTES + (pl * CV_NB + blk + 16) * 16) = w;
  }
  for (int it = tid; it < 4 * 264; it += 512) {
    int c = it / 264, qp = it % 264;
    int q = qp - 4;
    const u16* hp = ssm_k + (size_t)(d0 + c) * S_;
    u32x4 a = (u32x4){0u, 0u, 0u, 0u}, b = (u32x4){0u, 0u, 0u, 0u};
    if (q >= 0 && q < 256) a = *(const u32x4*)(hp + q * 8);
    if (q + 1 >= 0 && q + 1 < 256) b = *(const u32x4*)(hp + (q + 1) * 8);
    char* dst = hcs + c * CV_HC_BYTES + qp * 16;
    *(u32x4*)(dst + 0 * CV_CS8 * 8) = a;
    u32x4 s1;
    s1[0] = __builtin_amdgcn_alignbit(a[1], a[0], 16);
    s1[1] = __builtin_amdgcn_alignbit(a[2], a[1], 16);
    s1[2] = __builtin_amdgcn_alignbit(a[3], a[2], 16);
    s1[3] = __builtin_amdgcn_alignbit(b[0], a[3], 16);
    *(u32x4*)(dst + 1 * CV_CS8 * 8) = s1;
    u32x4 s2 = (u32x4){a[1], a[2], a[3], b[0]};
    *(u32x4*)(dst + 2 * CV_CS8 * 8) = s2;
    u32x4 s3;
    s3[0] = s1[1]; s3[1] = s1[2]; s3[2] = s1[3];
    s3[3] = __builtin_amdgcn_alignbit(b[1], b[0], 16);
    *(u32x4*)(dst + 3 * CV_CS8 * 8) = s3;
  }
  {
    int c = tid >> 7, pl = (tid >> 4) & 7, b = tid & 15;
    *(u32x4*)(kvs + c * CV_KV_BYTES + (pl * CV_NB + b) * 16) = (u32x4){0u, 0u, 0u, 0u};
  }
  float dd[4];
#pragma unroll
  for (int c = 0; c < 4; ++c) dd[c] = bf2f(ssm_D[d0 + c]);
  __syncthreads();

  const int ch = wave & 3, part = wave >> 2;
  const int c16 = lane & 15, g = lane >> 4;
  f32x4 accA[4], accB[4];
#pragma unroll
  for (int i = 0; i < 4; ++i) { accA[i] = (f32x4){0, 0, 0, 0}; accB[i] = (f32x4){0, 0, 0, 0}; }
  {
    const char* kvb = kvs + ch * CV_KV_BYTES + (g * CV_NB + 16 + c16) * 16;
    const int r = (c16 + 1) & 3, w = (c16 + 1) >> 2;
    const char* hb = hcs + ch * CV_HC_BYTES + (r * CV_CS8 - 2 * g + 6 + w) * 8;
    if (part == 0) {
      conv_part(hb, kvb + 16 * 16, 0, 24, accA);
    } else {
      conv_part(hb, kvb, 0, 16, accA);
      conv_part(hb, kvb + 16 * 16, 24, 32, accB);
#pragma unroll
      for (int i = 0; i < 4; ++i) *(f32x4*)(red + ((ch * 4 + i) * 64 + lane) * 16) = accB[i];
    }
  }
  __syncthreads();
  {
    char* yt = hcs + ch * 4096;
    const int ct = part ? 0 : 1;
#pragma unroll
    for (int i = 0; i < 4; ++i) {
      f32x4 a = accA[i];
      if (part == 0) a += *(const f32x4*)(red + ((ch * 4 + i) * 64 + lane) * 16);
      int t = 64 * (16 * ct + c16) + 16 * i + 4 * g;
      u32x2 w;
      w[0] = pack2(a[0], a[1]);
      w[1] = pack2(a[2], a[3]);
      *(u32x2*)(yt + t * 2) = w;
    }
  }
  __syncthreads();
  {
    u32x2 yc[4];
    float kvf[4][4];
    const int plane = (t0 & 63) >> 3, blk = (t0 >> 6) + 16, hi = (t0 >> 2) & 1;
#pragma unroll
    for (int c = 0; c < 4; ++c) {
      yc[c] = *(const u32x2*)(hcs + c * 4096 + t0 * 2);
      u32x2 kw = *(const u32x2*)(kvs + c * CV_KV_BYTES + (plane * CV_NB + blk) * 16 + (hi ? 0 : 8));
      kvf[c][3] = lo16(kw[0]); kvf[c][2] = hi16(kw[0]); kvf[c][1] = lo16(kw[1]); kvf[c][0] = hi16(kw[1]);
    }
#pragma unroll
    for (int i = 0; i < 4; ++i) {
      float o[4];
#pragma unroll
      for (int c = 0; c < 4; ++c) {
        unsigned w = yc[c][i >> 1];
        float yv = bf2f((u16)((i & 1) ? (w >> 16) : (w & 0xffff)));
        o[c] = yv + rbf(kvf[c][i] * dd[c]);
      }
      u32x2 w;
      w[0] = pack2(o[0], o[1]);
      w[1] = pack2(o[2], o[3]);
      *(u32x2*)(yout + (size_t)(t0 + i) * D_ + d0) = w;
    }
  }
  KEND(1);
}

template <int CTRL> __device__ __forceinline__ float dpp_add(float v) {
  int o = __builtin_amdgcn_update_dpp(0, __float_as_int(v), CTRL, 0xf, 0xf, true);
  return v + __int_as_float(o);
}
__device__ __forceinline__ float sum64(float v) {
  v = dpp_add<0xB1>(v);
  v = dpp_add<0x4E>(v);
  v = dpp_add<0x141>(v);
  v = dpp_add<0x140>(v);
  int iv = __float_as_int(v);
  float a = __int_as_float(__builtin_amdgcn_readlane(iv, 0)), b = __int_as_float(__builtin_amdgcn_readlane(iv, 16));
  float c = __int_as_float(__builtin_amdgcn_readlane(iv, 32)), d = __int_as_float(__builtin_amdgcn_readlane(iv, 48));
  return (a + b) + (c + d);
}
__device__ __forceinline__ void layernorm9(const float (&x)[9], float (&o)[9]) {
  float s = 0.f;
#pragma unroll
  for (int i = 0; i < 9; ++i) s += x[i];
  s = sum64(s);
  const float mean = s * (1.0f / D_);
  float q = 0.f;
#pragma unroll
  for (int i = 0; i < 9; ++i) { float d = x[i] - mean; q += d * d; }
  q = sum64(q);
  const float var = q * (1.0f / D_);
  const float mb = rbf(mean);
  const float vb = rbf(var);
  const float sd = rbf(sqrtf(rbf(vb + 1e-5f)));
#pragma unroll
  for (int i = 0; i < 9; ++i) o[i] = rbf(rbf(x[i] - mb) / sd);
}
__device__ __forceinline__ void load9(const u16* __restrict__ p, int l, float (&x)[9]) {
  u32x2 a = *(const u32x2*)(p + 4 * l), b = *(const u32x2*)(p + 256 + 4 * l);
  u16 c = p[512 + l];
  x[0] = lo16(a[0]); x[1] = hi16(a[0]); x[2] = lo16(a[1]); x[3] = hi16(a[1]);
  x[4] = lo16(b[0]); x[5] = hi16(b[0]); x[6] = lo16(b[1]); x[7] = hi16(b[1]);
  x[8] = bf2f(c);
}

template <int MODE, int KID>
__global__ __launch_bounds__(256) void row_kernel(const u16* __restrict__ xin, const float* __restrict__ part,
                                                  const u16* __restrict__ y, u16* __restrict__ xout,
                                                  u16* __restrict__ xn_t) {
  KSTART(KID);
  const int l = threadIdx.x & 63;
  const int row = blockIdx.x * 4 + (threadIdx.x >> 6);
  float x[9], yv[9];
  load9(xin + (size_t)row * D_, l, x);
  if (MODE <= 1) load9(y + (size_t)row * D_, l, yv);
  if (MODE >= 1) {
    f32x4 a = (f32x4){0, 0, 0, 0}, b = (f32x4){0, 0, 0, 0};
    float c = 0.f;
#pragma unroll
    for (int s = 0; s < NSPLIT; ++s) {
      const float* pp = part + ((size_t)s * S_ + row) * D_;
      a += *(const f32x4*)(pp + 4 * l);
      b += *(const f32x4*)(pp + 256 + 4 * l);
      c += pp[512 + l];
    }
#pragma unroll
    for (int e = 0; e < 4; ++e) { x[e] = rbf(x[e] + rbf(a[e])); x[4 + e] = rbf(x[4 + e] + rbf(b[e])); }
    x[8] = rbf(x[8] + rbf(c));
  }
  if (MODE <= 1) {
    float n[9];
    layernorm9(x, n);
#pragma unroll
    for (int i = 0; i < 9; ++i) x[i] = rbf(x[i] + rbf(n[i] * yv[i]));
  }
  {
    u16* po = xout + (size_t)row * D_;
    u32x2 w;
    w[0] = cvt2(x[0], x[1]); w[1] = cvt2(x[2], x[3]);
    *(u32x2*)(po + 4 * l) = w;
    w[0] = cvt2(x[4], x[5]); w[1] = cvt2(x[6], x[7]);
    *(u32x2*)(po + 256 + 4 * l) = w;
    po[512 + l] = f2bf(x[8]);
  }
  if (MODE <= 1) {
    float n[9];
    layernorm9(x, n);
    const int tt = row >> 4, c = row & 15;
    char* base = (char*)xn_t + (size_t)tt * KT1 * 1024 + c * 16;
    u32x2 w;
    w[0] = cvt2(n[0], n[1]); w[1] = cvt2(n[2], n[3]);
    *(u32x2*)(base + (l >> 3) * 1024 + (l & 3) * 256 + ((l >> 2) & 1) * 8) = w;
    w[0] = cvt2(n[4], n[5]); w[1] = cvt2(n[6], n[7]);
    *(u32x2*)(base + (8 + (l >> 3)) * 1024 + (l & 3) * 256 + ((l >> 2) & 1) * 8) = w;
    *(u16*)(base + (16 + (l >> 5)) * 1024 + ((l >> 2) & 3) * 256 + ((l >> 4) & 1) * 8 + (l & 3) * 2) = f2bf(n[8]);
  }
  KEND(KID);
}

static constexpr int STG = 34816;
static constexpr int NSTG = 4;

__device__ __forceinline__ u32x2 silu_mul4(f32x4 g, f32x4 u) {
  u32x2 o;
#pragma unroll
  for (int i = 0; i < 2; ++i) {
    unsigned gw = cvt2(g[2 * i], g[2 * i + 1]);
    unsigned uw = cvt2(u[2 * i], u[2 * i + 1]);
    float g0 = lo16(gw), g1 = hi16(gw);
    float s0 = g0 * __builtin_amdgcn_rcpf(1.0f + __builtin_amdgcn_exp2f(g0 * -1.44269504089f));
    float s1 = g1 * __builtin_amdgcn_rcpf(1.0f + __builtin_amdgcn_exp2f(g1 * -1.44269504089f));
    unsigned sw = cvt2(s0, s1);
    o[i] = cvt2(lo16(sw) * lo16(uw), hi16(sw) * hi16(uw));
  }
  return o;
}

template <int N> __device__ __forceinline__ void wait_vm2(bf16x8& a, bf16x8& b) {
  asm volatile("s_waitcnt vmcnt(%2)" : "+v"(a), "+v"(b) : "i"(N) : "memory");
}
__device__ __forceinline__ void wait_vm2_dyn(int n, bf16x8& a, bf16x8& b) {
  switch (n) {
    case 0: wait_vm2<0>(a, b); break;
    case 1: wait_vm2<1>(a, b); break;
    case 2: wait_vm2<2>(a, b); break;
    case 3: wait_vm2<3>(a, b); break;
    case 4: wait_vm2<4>(a, b); break;
    case 5: wait_vm2<5>(a, b); break;
    case 6: wait_vm2<6>(a, b); break;
    case 7: wait_vm2<7>(a, b); break;
    case 8: wait_vm2<8>(a, b); break;
    case 9: wait_vm2<9>(a, b); break;
    case 10: wait_vm2<10>(a, b); break;
    case 11: wait_vm2<11>(a, b); break;
    case 12: wait_vm2<12>(a, b); break;
    case 13: wait_vm2<13>(a, b); break;
    case 14: wait_vm2<14>(a, b); break;
    case 15: wait_vm2<15>(a, b); break;
    case 16: wait_vm2<16>(a, b); break;
    case 17: wait_vm2<17>(a, b); break;
    case 18: wait_vm2<18>(a, b); break;
    case 19: wait_vm2<19>(a, b); break;
    case 20: wait_vm2<20>(a, b); break;
    case 21: wait_vm2<21>(a, b); break;
    case 22: wait_vm2<22>(a, b); break;
    case 23: wait_vm2<23>(a, b); break;
    case 24: wait_vm2<24>(a, b); break;
    case 25: wait_vm2<25>(a, b); break;
    case 26: wait_vm2<26>(a, b); break;
    case 27: wait_vm2<27>(a, b); break;
    case 28: wait_vm2<28>(a, b); break;
    case 29: wait_vm2<29>(a, b); break;
    case 30: wait_vm2<30>(a, b); break;
    case 31: wait_vm2<31>(a, b); break;
    default: wait_vm2<0>(a, b); break;
  }
}
template <int N> __device__ __forceinline__ void wait_vm() {
  asm volatile("s_waitcnt vmcnt(%0)" :: "i"(N) : "memory");
}
__device__ __forceinline__ bf16x8 gload16(const void* p) {
  bf16x8 r;
  asm volatile("global_load_dwordx4 %0, %1, off" : "=v"(r) : "v"(p) : "memory");
  return r;
}
__device__ __forceinline__ u32x2 swiglu4(f32x4 g, f32x4 u) {
  f32x4 e;
#pragma unroll
  for (int i = 0; i < 4; ++i) e[i] = __builtin_amdgcn_exp2f(g[i] * -1.44269504089f);
  e = e + 1.0f;
#pragma unroll
  for (int i = 0; i < 4; ++i) e[i] = __builtin_amdgcn_rcpf(e[i]);
  f32x4 h = (g * u) * e;
  u32x2 o;
  o[0] = cvt2(h[0], h[1]);
  o[1] = cvt2(h[2], h[3]);
  return o;
}

template <int KID>
__global__ __launch_bounds__(512, 2) void gemm1_kernel(const u16* __restrict__ xn_t, const u16* __restrict__ w1,
                                                       const u16* __restrict__ w3, u16* __restrict__ h_t) {
  __shared__ __attribute__((aligned(16))) char lds[NSTG * STG];
  const int tid = threadIdx.x, lane = tid & 63;
  const int wave = __builtin_amdgcn_readfirstlane(tid >> 6);
  const int wm = wave & 3, wn = wave >> 2;
  const int bid = blockIdx.x, xcd = bid & 7, idx = bid >> 3;
  const int ft = xcd * 4 + (idx & 3), mt = idx >> 2;
  KSTART(KID);
  const char* xs = (const char*)xn_t + ((size_t)(mt * 16 + 2 * wave) * KT1) * 1024 + lane * 16;
  const char *ws0, *ws1, *ws2;
  {
    const int colA = 32 * (9 * (ft >> 1) + 4 * (ft & 1)), colB = 32 * (9 * (ft >> 1) + 8) + 16 * (ft & 1) - 128;
    int ci = wave * 128 + lane;
    int img = ci / 576, rem = ci % 576, row = rem / 18, cc = rem % 18;
    ws0 = (const char*)((img ? w3 : w1) + (size_t)row * F_ + (cc < 16 ? colA : colB) + cc * 8);
    ci += 64;
    img = ci / 576; rem = ci % 576; row = rem / 18; cc = rem % 18;
    ws1 = (const char*)((img ? w3 : w1) + (size_t)row * F_ + (cc < 16 ? colA : colB) + cc * 8);
    ci = 1024 + wave * 16 + (lane & 15);
    img = ci / 576; rem = ci % 576; row = rem / 18; cc = rem % 18;
    ws2 = (const char*)((img ? w3 : w1) + (size_t)row * F_ + (cc < 16 ? colA : colB) + cc * 8);
  }
  constexpr size_t WSTEP = (size_t)32 * F_ * 2;
  const unsigned lbase = lds_addr(lds);
  auto issue = [&](int s) {
    unsigned sb = lbase + (s & 3) * STG;
    glds16(xs + s * 1024, sb + (2 * wave) * 1024);
    glds16(xs + KT1 * 1024 + s * 1024, sb + (2 * wave + 1) * 1024);
    glds16(ws0 + s * WSTEP, sb + 16384 + wave * 2048);
    glds16(ws1 + s * WSTEP, sb + 16384 + wave * 2048 + 1024);
    if (lane < 16) glds16(ws2 + s * WSTEP, sb + 16384 + 16384 + wave * 256);
  };
  auto issueP = [&](int s, int part_) {
    unsigned sb = lbase + (s & 3) * STG;
    if (part_ == 0) glds16(xs + s * 1024, sb + (2 * wave) * 1024);
    if (part_ == 1) glds16(xs + KT1 * 1024 + s * 1024, sb + (2 * wave + 1) * 1024);
    if (part_ == 2) glds16(ws0 + s * WSTEP, sb + 16384 + wave * 2048);
    if (part_ == 3) glds16(ws1 + s * WSTEP, sb + 16384 + wave * 2048 + 1024);
    if (part_ == 4) { if (lane < 16) glds16(ws2 + s * WSTEP, sb + 16384 + 16384 + wave * 256); }
  };

  f32x4 acc[9][4];
#pragma unroll
  for (int n = 0; n < 9; ++n)
#pragma unroll
    for (int m = 0; m < 4; ++m) acc[n][m] = (f32x4){0, 0, 0, 0};

  const int g = lane >> 4, q = (lane & 15) >> 2, p = lane & 3;
  const int cb = wn * 64, cm = 128 + 8 * wn;
  const int xoff = wm * 4096 + lane * 16;
  const int woff = 16384 + (4 * g + q) * 288 + (cb + 4 * p) * 2;
  const int moff = 16384 + (p >> 1) * 9216 + (4 * g + q) * 288 + (cm + 4 * (p & 1)) * 2;

  issue(0); issue(1); issue(2);
  auto wread = [&](const char* sb, int n) -> bf16x8 {
    const char* wp = (n < 8) ? (sb + woff + (n >> 2) * 9216 + (n & 3) * 32) : (sb + moff);
    return cat8(tr_read(wp), tr_read(wp + 16 * 288));
  };
  bf16x8 xa[4], xb[4], fa, fb, fc;
  WAIT_V(10);
  __builtin_amdgcn_s_barrier();
  asm volatile("" ::: "memory");
#pragma unroll
  for (int m = 0; m < 4; ++m) xa[m] = *(const bf16x8*)(lds + xoff + m * 1024);
  fa = wread(lds, 0);
  fb = wread(lds, 1);

#define SB0 __builtin_amdgcn_sched_barrier(0)
#define MM(n, w, x) _Pragma("unroll") for (int m = 0; m < 4; ++m) acc[n][m] = __builtin_amdgcn_mfma_f32_16x16x32_bf16(w, x[m], acc[n][m], 0, 0, 0)
  auto body = [&](int s, bf16x8 (&xc)[4], bf16x8 (&xn)[4], bool last) {
    const char* sb = lds + (s & 3) * STG;
    const char* sn = lds + ((s + 1) & 3) * STG;
    fc = wread(sb, 2); SB0;
    MM(0, fa, xc); SB0; fa = wread(sb, 3); SB0;
    if (s >= 1 && s + 2 < KT1) issueP(s + 2, 3);
    SB0;
    MM(1, fb, xc); SB0; fb = wread(sb, 4); SB0;
    if (s >= 1 && s + 2 < KT1) issueP(s + 2, 4);
    SB0;
    MM(2, fc, xc); SB0; fc = wread(sb, 5); SB0;
    MM(3, fa, xc); SB0; fa = wread(sb, 6); SB0;
    MM(4, fb, xc); SB0; fb = wread(sb, 7); SB0;
    MM(5, fc, xc); SB0; fc = wread(sb, 8); SB0;
    if (!last) {
      if (s < KT1 - 2) WAIT_V(5); else WAIT_V(0);
      __builtin_amdgcn_s_barrier();
      asm volatile("" ::: "memory");
#pragma unroll
      for (int m = 0; m < 4; ++m) xn[m] = *(const bf16x8*)(sn + xoff + m * 1024);
      SB0;
    }
    if (s + 3 < KT1) issueP(s + 3, 0);
    SB0;
    MM(6, fa, xc); SB0; if (!last) fa = wread(sn, 0); SB0;
    if (s + 3 < KT1) issueP(s + 3, 1);
    SB0;
    MM(7, fb, xc); SB0; if (!last) fb = wread(sn, 1); SB0;
    if (s + 3 < KT1) issueP(s + 3, 2);
    SB0;
    MM(8, fc, xc); SB0;
  };
  for (int s = 0; s < KT1 - 2; s += 2) {
    body(s, xa, xb, false);
    body(s + 1, xb, xa, false);
  }
  body(KT1 - 2, xa, xb, false);
  body(KT1 - 1, xb, xa, true);
#undef MM

  const int c = lane & 15;
  const int P0 = 9 * (ft >> 1) + 4 * (ft & 1) + 2 * wn;
  const int PX = 9 * (ft >> 1) + 8;
#pragma unroll
  for (int m = 0; m < 4; ++m) {
    const int tt = mt * 16 + wm * 4 + m;
    char* hb = (char*)h_t + (size_t)tt * KT2 * 1024;
#pragma unroll
    for (int n = 0; n < 2; ++n) {
      u32x2 a = swiglu4(acc[2 * n][m], acc[4 + 2 * n][m]);
      u32x2 b = swiglu4(acc[2 * n + 1][m], acc[5 + 2 * n][m]);
      st16u(hb + (size_t)(P0 + n) * 1024 + lane * 16, (u32x4){a[0], a[1], b[0], b[1]});
    }
    {
      f32x4 o;
#pragma unroll
      for (int r = 0; r < 4; ++r) o[r] = __shfl_xor(acc[8][m][r], 32, 64);
      if (lane < 32) {
        u32x2 w = swiglu4(acc[8][m], o);
        int gs = 2 * wn + g;
        *(u32x2*)(hb + (size_t)PX * 1024 + (gs * 16 + c) * 16 + (ft & 1) * 8) = w;
      }
    }
  }
  KEND(KID);
}

static constexpr int WST = 18432;
template <int KID>
__global__ __launch_bounds__(512, 2) void gemm2_kernel(const u16* __restrict__ h_t, const u16* __restrict__ w2,
                                                       float* __restrict__ part) {
  __shared__ __attribute__((aligned(16))) char lds[8 * WST];
  const int tid = threadIdx.x, lane = tid & 63;
  const int wave = __builtin_amdgcn_readfirstlane(tid >> 6);
  const int wm = wave & 3, wk = wave >> 2;
  const int bid = blockIdx.x, xcd = bid & 7, idx = bid >> 3;
  const int ks = xcd >> 1;
  const int nt = idx & 3, mt = (xcd & 1) * 8 + (idx >> 2);
  const int kt0 = ks * 36;
  KSTART(KID);

  const char* xs0 = (const char*)h_t + ((size_t)(mt * 8 + wm * 2) * KT2 + kt0 + wk * 18) * 1024 + lane * 16;
  const char* xs1 = xs0 + (size_t)KT2 * 1024;
  const char *ws0, *ws1, *ws2;
  {
    int ci = wave * 128 + lane;
    int img = ci / 576, rem = ci % 576, row = rem / 18, cc = rem % 18;
    ws0 = (const char*)(w2 + (size_t)((kt0 + img * 18) * 32 + row) * D_ + nt * 144 + cc * 8);
    ci += 64;
    img = ci / 576; rem = ci % 576; row = rem / 18; cc = rem % 18;
    ws1 = (const char*)(w2 + (size_t)((kt0 + img * 18) * 32 + row) * D_ + nt * 144 + cc * 8);
    ci = 1024 + wave * 16 + (lane & 15);
    img = ci / 576; rem = ci % 576; row = rem / 18; cc = rem % 18;
    ws2 = (const char*)(w2 + (size_t)((kt0 + img * 18) * 32 + row) * D_ + nt * 144 + cc * 8);
  }
  constexpr size_t WSTEP = (size_t)32 * D_ * 2;
  const unsigned lbase = lds_addr(lds);
  auto issueW0 = [&](int s) {
    glds16(ws0, lbase + (s & 7) * WST + wave * 2048);
    ws0 += WSTEP; asm volatile("" : "+v"(ws0));
  };
  auto issueW1 = [&](int s) {
    glds16(ws1, lbase + (s & 7) * WST + wave * 2048 + 1024);
    ws1 += WSTEP; asm volatile("" : "+v"(ws1));
  };
  auto issueW2 = [&](int s) {
    if (lane < 16) glds16(ws2, lbase + (s & 7) * WST + 16384 + wave * 256);
    ws2 += WSTEP; asm volatile("" : "+v"(ws2));
  };
  auto issueW = [&](int s) { issueW0(s); issueW1(s); issueW2(s); };
  auto loadX0 = [&](bf16x8 (&x)[2]) { x[0] = gload16(xs0); xs0 += 1024; asm volatile("" : "+v"(xs0)); };
  auto loadX1 = [&](bf16x8 (&x)[2]) { x[1] = gload16(xs1); xs1 += 1024; asm volatile("" : "+v"(xs1)); };
  auto loadX = [&](bf16x8 (&x)[2]) { loadX0(x); loadX1(x); };

  f32x4 acc[9][2];
#pragma unroll
  for (int n = 0; n < 9; ++n) { acc[n][0] = (f32x4){0, 0, 0, 0}; acc[n][1] = (f32x4){0, 0, 0, 0}; }

  const int g = lane >> 4, q = (lane & 15) >> 2, p = lane & 3;
  const int woff = wk * 9216 + (4 * g + q) * 288 + (4 * p) * 2;

  bf16x8 xr[6][2];
#pragma unroll
  for (int j = 0; j < 5; ++j) { loadX(xr[j]); issueW(j); }
  issueW(5); issueW(6);
  wait_vm2<3 + 4 * 5 + 6>(xr[0][0], xr[0][1]);
  __builtin_amdgcn_s_barrier();
  asm volatile("" ::: "memory");

  auto wread = [&](const char* sb, int n) -> bf16x8 {
    const char* wp = sb + n * 32;
    return cat8(tr_read(wp), tr_read(wp + 16 * 288));
  };
#define SB0 __builtin_amdgcn_sched_barrier(0)
#define MM(n, w, x) do { acc[n][0] = __builtin_amdgcn_mfma_f32_16x16x32_bf16(w, x[0], acc[n][0], 0, 0, 0); \
                         acc[n][1] = __builtin_amdgcn_mfma_f32_16x16x32_bf16(w, x[1], acc[n][1], 0, 0, 0); } while (0)
  bf16x8 fa = wread(lds + woff, 0), fb = wread(lds + woff, 1), fc;
  auto stage = [&](int s, bf16x8 (&xc)[2], bf16x8 (&xn)[2], bf16x8 (&xp)[2], bool last) {
    const char* sb = lds + (s & 7) * WST + woff;
    const char* sn = lds + ((s + 1) & 7) * WST + woff;
    fc = wread(sb, 2); SB0;
    MM(0, fa, xc); fa = wread(sb, 3); SB0;
    if (s >= 1 && s + 6 < 18) issueW1(s + 6);
    SB0;
    MM(1, fb, xc); fb = wread(sb, 4); SB0;
    if (s >= 1 && s + 6 < 18) issueW2(s + 6);
    SB0;
    MM(2, fc, xc); fc = wread(sb, 5); SB0;
    MM(3, fa, xc); fa = wread(sb, 6); SB0;
    MM(4, fb, xc); fb = wread(sb, 7); SB0;
    MM(5, fc, xc); fc = wread(sb, 8); SB0;
    if (!last) {
      if (s <= 11) wait_vm<18>();
      else if (s == 12) wait_vm<15>();
      else if (s == 13) wait_vm<12>();
      else if (s == 14) wait_vm<7>();
      else if (s == 15) wait_vm<2>();
      else wait_vm<0>();
      asm volatile("" : "+v"(xn[0]), "+v"(xn[1]));
      __builtin_amdgcn_s_barrier();
      asm volatile("" ::: "memory");
      SB0;
    }
    if (s + 5 < 18) loadX0(xp);
    SB0;
    MM(6, fa, xc); if (!last) fa = wread(sn, 0); SB0;
    if (s + 5 < 18) loadX1(xp);
    SB0;
    MM(7, fb, xc); if (!last) fb = wread(sn, 1); SB0;
    if (s + 7 < 18) issueW0(s + 7);
    SB0;
    MM(8, fc, xc); SB0;
  };
#pragma unroll 1
  for (int s = 0; s < 18; s += 6) {
    stage(s + 0, xr[0], xr[1], xr[5], false);
    stage(s + 1, xr[1], xr[2], xr[0], false);
    stage(s + 2, xr[2], xr[3], xr[1], false);
    stage(s + 3, xr[3], xr[4], xr[2], false);
    stage(s + 4, xr[4], xr[5], xr[3], false);
    stage(s + 5, xr[5], xr[0], xr[4], s + 5 == 17);
  }
#undef MM
  BAR();
  f32x4* red = (f32x4*)lds;
  int tz = threadIdx.x; asm volatile("" : "+v"(tz));
  const int ln = tz & 63;
#pragma unroll
  for (int n = 0; n < 9; ++n) {
    if ((n < 5) == (wk == 1)) {
#pragma unroll
      for (int m = 0; m < 2; ++m) red[(wm * 18 + n * 2 + m) * 64 + ln] = acc[n][m];
    }
  }
  BAR();
  {
    const int c = ln & 15;
#pragma unroll
    for (int m = 0; m < 2; ++m) {
      const int token = mt * 128 + wm * 32 + m * 16 + c;
      float* dst = part + ((size_t)ks * S_ + token) * D_ + nt * 144 + 4 * (ln >> 4);
#pragma unroll
      for (int n = 0; n < 9; ++n) {
        if ((n < 5) == (wk == 0)) {
          f32x4 o = acc[n][m] + red[(wm * 18 + n * 2 + m) * 64 + ln];
          st16(dst + n * 16, o);
        }
      }
    }
  }
  KEND(KID);
}

extern "C" void kernel_launch(void* const* d_in, const int* in_sizes, int n_in,
                              void* d_out, int out_size, void* d_ws, size_t ws_size,
                              hipStream_t stream) {
  const u16* A_q = (const u16*)d_in[0];
  const u16* A_k = (const u16*)d_in[1];
  const u16* A_v = (const u16*)d_in[2];
  const u16* A_shift = (const u16*)d_in[3];
  const u16* A_ssmk = (const u16*)d_in[4];
  const u16* A_ssmD = (const u16*)d_in[5];
  const u16* B_w1 = (const u16*)d_in[7];
  const u16* B_w3 = (const u16*)d_in[8];
  const u16* B_w2 = (const u16*)d_in[9];
  u16* out = (u16*)d_out;

  char* ws = (char*)d_ws;
  constexpr size_t XB = (size_t)S_ * D_ * 2;
  u16* y = (u16*)(ws);
  u16* xa = (u16*)(ws + XB);
  u16* xb = (u16*)(ws + 2 * XB);
  u16* xn = (u16*)(ws + 3 * XB);
  u16* h = (u16*)(ws + 4 * XB);
  float* part = (float*)(ws + 4 * XB + (size_t)S_ * F_ * 2);

  kvt_kernel<<<288, 256, 0, stream>>>(A_k, A_v, A_shift, xb);
  conv_kernel<<<256, 512, 0, stream>>>(xb, A_ssmk, A_ssmD, y, B_w1, B_w3, B_w2);
  row_kernel<0, KIDV(2)><<<S_ / 4, 256, 0, stream>>>(A_q, nullptr, y, xa, xn);
  gemm1_kernel<KIDV(3)><<<256, 512, 0, stream>>>(xn, B_w1, B_w3, h);
  gemm2_kernel<KIDV(4)><<<256, 512, 0, stream>>>(h, B_w2, part);
  row_kernel<1, KIDV(5)><<<S_ / 4, 256, 0, stream>>>(xa, part, y, xb, xn);
  gemm1_kernel<KIDV(6)><<<256, 512, 0, stream>>>(xn, B_w1, B_w3, h);
  gemm2_kernel<KIDV(7)><<<256, 512, 0, stream>>>(h, B_w2, part);
  row_kernel<2, KIDV(8)><<<S_ / 4, 256, 0, stream>>>(xb, part, nullptr, out, nullptr);
}
```
